```python
import torch

def _softmax(x, axis=-1):
    return torch.softmax(x, dim=axis)
def _gelu(x):
    return torch.nn.functional.gelu(x, approximate="tanh")
def _silu(x):
    return torch.nn.functional.silu(x)
def _sigmoid(x):
    return torch.sigmoid(x)
def _layernorm(x, axis=-1, eps=1e-5):
    m = x.mean(dim=axis, keepdim=True)
    v = x.var(dim=axis, keepdim=True, unbiased=False)
    return (x - m) / torch.sqrt(v + eps)
def _scatter_add(tgt, idx, src, axis=0):
    return tgt.clone().index_add_(axis, idx, src)
def _segment_sum(src, seg, n):
    out = torch.zeros((n,) + src.shape[1:], dtype=src.dtype)
    return out.index_add_(0, seg, src)
def _pad(x, pw):
    flat = tuple(v for pair in reversed(pw) for v in pair)
    return torch.nn.functional.pad(x, flat)

kernel_name = 'parallel_sum__ssm_conv_h3__glu_gated_bilinear__prompt'

def setup_inputs():

    _w = (lambda a: (lambda t: t.to(torch.bfloat16) if t.is_floating_point() else t)(torch.as_tensor(a)))
    def _A_setup():
        B, S, D = 1, 2048, 512
        ks = 4
        return {
            "q": _w((lambda *s: torch.randn(*s, dtype=torch.bfloat16) * 0.05)(B, S, D)),
            "k": _w((lambda *s: torch.randn(*s, dtype=torch.bfloat16) * 0.2)(B, S, D)),
            "v": _w((lambda *s: torch.randn(*s, dtype=torch.bfloat16) * 0.2)(B, S, D)),
            "shift_k": _w((lambda *s: torch.randn(*s, dtype=torch.bfloat16) * 0.2)(D, ks) * 0.5),
            "ssm_k": _w((lambda *s: torch.randn(*s, dtype=torch.bfloat16) * 0.4)(D, S) * (1.0 / S ** 0.5)),
            "ssm_D": _w((lambda *s: torch.randn(*s, dtype=torch.bfloat16) * 0.2)(D)),
        }
    _A_in = _A_setup()

    def _B_setup():
        B, S, D, F = 1, 2048, 512, 1376
        return {
            "x": _w((lambda *s: torch.randn(*s, dtype=torch.bfloat16) * 0.05)(B, S, D)),
            "w_gate": _w((lambda *s: torch.randn(*s, dtype=torch.bfloat16) * 0.2)(D, F) * (D ** -0.5)),
            "w_up": _w((lambda *s: torch.randn(*s, dtype=torch.bfloat16) * 0.2)(D, F) * (D ** -0.5)),
            "w_down": _w((lambda *s: torch.randn(*s, dtype=torch.bfloat16) * 0.2)(F, D) * (F ** -0.5)),
        }
    _B_in = _B_setup()

    return {
        "A_q": _A_in["q"],
        "A_k": _A_in["k"],
        "A_v": _A_in["v"],
        "A_shift_k": _A_in["shift_k"],
        "A_ssm_k": _A_in["ssm_k"],
        "A_ssm_D": _A_in["ssm_D"],
        "B_x": _B_in["x"],
        "B_w_gate": _B_in["w_gate"],
        "B_w_up": _B_in["w_up"],
        "B_w_down": _B_in["w_down"],
    }

def reference(A_q, A_k, A_v, A_shift_k, A_ssm_k, A_ssm_D, B_x, B_w_gate, B_w_up, B_w_down):
    def _A_ref(q, k, v, shift_k, ssm_k, ssm_D):
        B, S, D = q.shape
        ks = shift_k.shape[-1]
        k_pad = _pad(k, ((0, 0), (ks - 1, 0), (0, 0)))
        k_shift = torch.zeros_like(k)
        for t in range(ks):
            k_shift = k_shift + k_pad[:, t:t + S, :] * shift_k[:, ks - 1 - t][None, None, :]
        kv = k_shift * v
        L = 2 * S
        kv_pad = _pad(kv, ((0, 0), (0, S), (0, 0)))
        kf_pad = _pad(ssm_k, ((0, 0), (0, S)))
        kv_f = (lambda x, axes=(-2,-1): torch.fft.rfft2(x.float(), dim=axes))(kv_pad[:, None, :, :], axes=(1, 2))
        kf_f = (lambda x, axes=(-2,-1): torch.fft.rfft2(x.float(), dim=axes))(kf_pad[:, None, :], axes=(1, 2))
        kf_f = (lambda x, *d: x.permute(*d))(kf_f, 1, 2, 0)
        y_f = kv_f * kf_f[None, :, :, :]
        y = (lambda x, s, axes=(-2,-1): torch.fft.irfft2(x, s=s, dim=axes).to(torch.bfloat16))(y_f, (1, L), axes=(1, 2))[:, 0, :S, :]
        y = y + kv * ssm_D[None, None, :]
        return q * y

    def _B_ref(x, w_gate, w_up, w_down):
        g = torch.einsum("bsd,df->bsf", x, w_gate)
        u = torch.einsum("bsd,df->bsf", x, w_up)
        h = g * u
        return torch.einsum("bsf,fd->bsd", h, w_down)

    x = A_q
    xn = _layernorm(x, axis=-1)
    return x + _A_ref(xn, A_k, A_v, A_shift_k, A_ssm_k, A_ssm_D) + _B_ref(xn, B_w_gate, B_w_up, B_w_down)
```

```cpp
#include <hip/hip_runtime.h>
#include <hip/hip_bf16.h>
typedef __hip_bfloat16 bf16;
#define S 2048
#define D 512
#define F 1376
__device__ __forceinline__ float rb(float x){ return __bfloat162float(__float2bfloat16(x)); }
__global__ void k_ln(const bf16* x, const bf16* k, const bf16* v, const bf16* sk, bf16* xn, bf16* kv){
  int t = blockIdx.x; int d = threadIdx.x;
  __shared__ float red[512];
  float xv = __bfloat162float(x[t*D+d]);
  red[d]=xv; __syncthreads();
  for(int s=256;s>0;s>>=1){ if(d<s) red[d]+=red[d+s]; __syncthreads(); }
  float mean = red[0]/D; __syncthreads();
  float dd = xv-mean; red[d]=dd*dd; __syncthreads();
  for(int s=256;s>0;s>>=1){ if(d<s) red[d]+=red[d+s]; __syncthreads(); }
  float var = red[0]/D;
  float mb = rb(mean), vb = rb(var);
  float sd = rb(sqrtf(rb(vb+1e-5f)));
  xn[t*D+d] = __float2bfloat16(rb(xv-mb)/sd);
  float acc=0.f;
  for(int j=3;j>=0;--j){ float kk = (t-j>=0)? __bfloat162float(k[(t-j)*D+d]):0.f; acc = rb(acc + rb(kk*__bfloat162float(sk[d*4+j]))); }
  kv[t*D+d] = __float2bfloat16(acc*__bfloat162float(v[t*D+d]));
}
__global__ void k_conv(const bf16* kv, const bf16* ssk, const bf16* sD, bf16* ya){
  int t = blockIdx.x; int d = threadIdx.x;
  float acc=0.f;
  for(int r=0;r<=t;++r) acc += __bfloat162float(kv[(t-r)*D+d])*__bfloat162float(ssk[d*S+r]);
  float y = rb(acc);
  ya[t*D+d] = __float2bfloat16(y + rb(__bfloat162float(kv[t*D+d])*__bfloat162float(sD[d])));
}
__global__ void k_g1(const bf16* xn, const bf16* wg, const bf16* wu, bf16* h){
  int t = blockIdx.x;
  for(int f=threadIdx.x; f<F; f+=blockDim.x){
    float g=0,u=0;
    for(int k=0;k<D;++k){ float a=__bfloat162float(xn[t*D+k]); g+=a*__bfloat162float(wg[k*F+f]); u+=a*__bfloat162float(wu[k*F+f]); }
    h[t*F+f]=__float2bfloat16(rb(g)*rb(u));
  }
}
__global__ void k_g2(const bf16* h, const bf16* wd, const bf16* x, const bf16* xn, const bf16* ya, bf16* out){
  int t = blockIdx.x; int d=threadIdx.x;
  float o=0; for(int f=0;f<F;++f) o+=__bfloat162float(h[t*F+f])*__bfloat162float(wd[f*D+d]);
  float a = rb(__bfloat162float(xn[t*D+d])*__bfloat162float(ya[t*D+d]));
  out[t*D+d]=__float2bfloat16(rb(__bfloat162float(x[t*D+d])+a)+rb(o));
}
extern "C" void kernel_launch(void* const* d_in, const int* in_sizes, int n_in, void* d_out, int out_size, void* d_ws, size_t ws_size, hipStream_t stream){
  const bf16* q=(const bf16*)d_in[0]; const bf16* k=(const bf16*)d_in[1]; const bf16* v=(const bf16*)d_in[2];
  const bf16* sk=(const bf16*)d_in[3]; const bf16* ssk=(const bf16*)d_in[4]; const bf16* sD=(const bf16*)d_in[5];
  const bf16* wg=(const bf16*)d_in[7]; const bf16* wu=(const bf16*)d_in[8]; const bf16* wd=(const bf16*)d_in[9];
  bf16* xn=(bf16*)d_ws; bf16* kv=xn+S*D; bf16* ya=kv+S*D; bf16* h=ya+S*D;
  k_ln<<<S,512,0,stream>>>(q,k,v,sk,xn,kv);
  k_conv<<<S,512,0,stream>>>(kv,ssk,sD,ya);
  k_g1<<<S,256,0,stream>>>(xn,wg,wu,h);
  k_g2<<<S,512,0,stream>>>(h,wd,q,xn,ya,(bf16*)d_out);
}
```

```cpp
#include <hip/hip_runtime.h>
#include <hip/hip_bf16.h>
#include <stdint.h>

typedef __hip_bfloat16 bf16;
typedef short v4s __attribute__((ext_vector_type(4)));
typedef short v8s __attribute__((ext_vector_type(8)));
typedef __bf16 v2bf __attribute__((ext_vector_type(2)));
typedef __bf16 v8bf __attribute__((ext_vector_type(8)));
typedef float f32x4 __attribute__((ext_vector_type(4)));
typedef float f32x2 __attribute__((ext_vector_type(2)));
typedef unsigned v4u __attribute__((ext_vector_type(4)));
typedef unsigned v2u __attribute__((ext_vector_type(2)));
#define LAS __attribute__((address_space(3)))
#define GAS __attribute__((address_space(1)))

constexpr int S_ = 2048, D_ = 512, F_ = 1376;

__device__ __forceinline__ float bflo(unsigned w) { return __uint_as_float(w << 16); }
__device__ __forceinline__ float bfhi(unsigned w) { return __uint_as_float(w & 0xffff0000u); }
__device__ __forceinline__ unsigned pk2(float a, float b) {
  v2bf p = {(__bf16)a, (__bf16)b};
  return __builtin_bit_cast(unsigned, p);
}
__device__ __forceinline__ float rbf(float a) { return bflo(pk2(a, a)); }
__device__ __forceinline__ float dot2(unsigned a, unsigned b, float c) {
  return __builtin_amdgcn_fdot2_f32_bf16(__builtin_bit_cast(v2bf, a), __builtin_bit_cast(v2bf, b), c, false);
}
__device__ __forceinline__ v8s trfrag(const char* p) {
  v4s a = __builtin_amdgcn_ds_read_tr16_b64_v4i16((LAS v4s*)(p));
  v4s b = __builtin_amdgcn_ds_read_tr16_b64_v4i16((LAS v4s*)(p + 256));
  return __builtin_shufflevector(a, b, 0, 1, 2, 3, 4, 5, 6, 7);
}
__device__ __forceinline__ f32x4 mfma16(v8s a, v8s b, f32x4 c) {
  return __builtin_amdgcn_mfma_f32_16x16x32_bf16(__builtin_bit_cast(v8bf, a), __builtin_bit_cast(v8bf, b), c, 0, 0, 0);
}
__device__ __forceinline__ void glds16(const void* g, char* l) {
  const unsigned la = (unsigned)__builtin_amdgcn_readfirstlane((unsigned)(uintptr_t)(LAS char*)l);
  unsigned keep;
  asm volatile("s_mov_b32 %0, m0\n\ts_mov_b32 m0, %2\n\ts_nop 0\n\tglobal_load_lds_dwordx4 %1, off\n\ts_mov_b32 m0, %0"
               : "=&s"(keep) : "v"(g), "s"(la) : "memory");
}
#define WAIT_V(n) asm volatile("s_waitcnt vmcnt(" #n ")" ::: "memory")
#define RAW_BAR() do { asm volatile("s_waitcnt lgkmcnt(0)" ::: "memory"); __builtin_amdgcn_s_barrier(); } while (0)

constexpr int K1_STAGE = 40960;
constexpr int K1_NBUF = 3;
constexpr int K1_EXTRA = K1_NBUF * K1_STAGE;
constexpr int K1_LDS = K1_EXTRA + 4096;

__global__ __launch_bounds__(512, 2) void k1_gemm1(const bf16* __restrict__ x, const bf16* __restrict__ kin,
                                                   const bf16* __restrict__ vin, const bf16* __restrict__ shiftk,
                                                   const bf16* __restrict__ wg, const bf16* __restrict__ wu,
                                                   bf16* __restrict__ h, bf16* __restrict__ kvT,
                                                   float* __restrict__ stats, unsigned* __restrict__ cnt) {
  extern __shared__ __attribute__((aligned(16))) char sm[];
  const int tid = threadIdx.x, lane = tid & 63;
  const int w = __builtin_amdgcn_readfirstlane(tid >> 6);
  const int b = blockIdx.x, xcd = b & 7, idx = b >> 3;
  const int rt = (xcd >> 2) * 8 + (idx & 7), ct = (xcd & 3) * 4 + (idx >> 3);
  const bool gemm = ct < 15;
  if (b == 0 && tid < 8) cnt[tid * 64] = 0u;

  const int t0 = rt * 128, f0 = ct * 96;
  const char* xsrc0;
  const char* xsrc1;
  const char* wsrc;
  {
    const int j0 = 2 * w, j1 = 2 * w + 1;
    const int r0 = 8 * j0 + (lane >> 3), r1 = 8 * j1 + (lane >> 3);
    xsrc0 = (const char*)x + (size_t)(t0 + r0) * 1024 + (((lane & 7) ^ ((r0 >> 1) & 7)) << 4);
    xsrc1 = (const char*)x + (size_t)(t0 + r1) * 1024 + (((lane & 7) ^ ((r1 >> 1) & 7)) << 4);
    const int s = w >> 2, kg = w & 3;
    const int ftl = lane >> 5, kpos = (lane >> 1) & 15, half = lane & 1;
    const int k16 = (kpos & 3) | ((kpos & 4) << 1) | ((kpos & 8) >> 1);
    wsrc = (const char*)(s ? wu : wg) + (size_t)(16 * kg + k16) * (F_ * 2) + (size_t)(f0 + 16 * ftl + 8 * half) * 2;
  }
  const int fpstep = (ct == 14) ? 0 : 64;
  auto stage = [&](int kt, int buf) {
    char* base = sm + buf * K1_STAGE;
    glds16(xsrc0 + kt * 128, base + (2 * w) * 1024);
    glds16(xsrc1 + kt * 128, base + (2 * w + 1) * 1024);
    const char* ws_ = wsrc + (size_t)kt * (64 * F_ * 2);
    char* wb = base + 16384 + (w >> 2) * 12288 + (w & 3) * 3072;
    glds16(ws_, wb);
    glds16(ws_ + fpstep, wb + 1024);
    glds16(ws_ + 2 * fpstep, wb + 2048);
  };
  if (gemm) { stage(0, 0); stage(1, 1); stage(2, 2); }

  {
    const int tb0 = (b >> 3) * 64, c0 = (b & 7) * 64;
    const int cp = tid & 31, tg = tid >> 5;
    const int t = tb0 + 4 * tg, ch = c0 + 2 * cp;
    unsigned kk[7], vv[4];
#pragma unroll
    for (int i = 0; i < 7; ++i) {
      const int tt = t - 3 + i;
      kk[i] = (tt >= 0) ? *(const unsigned*)(kin + (size_t)tt * D_ + ch) : 0u;
    }
#pragma unroll
    for (int i = 0; i < 4; ++i) vv[i] = *(const unsigned*)(vin + (size_t)(t + i) * D_ + ch);
    const v4u sk = *(const v4u*)(shiftk + ch * 4);
    float w0[4] = {bflo(sk.x), bfhi(sk.x), bflo(sk.y), bfhi(sk.y)};
    float w1[4] = {bflo(sk.z), bfhi(sk.z), bflo(sk.w), bfhi(sk.w)};
    float r0[4], r1[4];
#pragma unroll
    for (int i = 0; i < 4; ++i) {
      unsigned acc = pk2(bflo(kk[i]) * w0[3], bfhi(kk[i]) * w1[3]);
#pragma unroll
      for (int j = 2; j >= 0; --j) {
        const unsigned p = pk2(bflo(kk[i + 3 - j]) * w0[j], bfhi(kk[i + 3 - j]) * w1[j]);
        acc = pk2(bflo(acc) + bflo(p), bfhi(acc) + bfhi(p));
      }
      r0[i] = bflo(acc) * bflo(vv[i]);
      r1[i] = bfhi(acc) * bfhi(vv[i]);
    }
    const int pos = (t & ~31) + 28 - (t & 31);
    v2u o0 = {pk2(r0[3], r0[2]), pk2(r0[1], r0[0])};
    v2u o1 = {pk2(r1[3], r1[2]), pk2(r1[1], r1[0])};
    *(v2u*)(kvT + (size_t)ch * S_ + pos) = o0;
    *(v2u*)(kvT + (size_t)(ch + 1) * S_ + pos) = o1;
  }
  if (!gemm) return;

  const int tw = w & 3, fw = w >> 2;
  const int q = lane >> 4, r16 = lane & 15;
  const int aoff = 16384 + (q >> 1) * 3072 + ((((q & 1) << 2) | (r16 >> 2)) << 5) + ((lane & 3) << 3) + fw * 1536;
  int xoff[2][2];
#pragma unroll
  for (int ks = 0; ks < 2; ++ks)
#pragma unroll
    for (int n = 0; n < 2; ++n)
      xoff[ks][n] = (32 * tw + 16 * (n ^ fw) + r16) * 128 + (((4 * ks + q) ^ (r16 >> 1)) << 4);

  f32x4 acc[2][3][2];
#pragma unroll
  for (int s = 0; s < 2; ++s)
#pragma unroll
    for (int ft = 0; ft < 3; ++ft)
#pragma unroll
      for (int n = 0; n < 2; ++n) acc[s][ft][n] = (f32x4){0.f, 0.f, 0.f, 0.f};
  float s1 = 0.f, s2 = 0.f;
  float cs[2][3] = {{0.f, 0.f, 0.f}, {0.f, 0.f, 0.f}};
  const unsigned ONE2 = 0x3f803f80u;

  int buf = 0;
  for (int kt = 0; kt < 8; ++kt) {
    if (kt < 7) { WAIT_V(5); } else { WAIT_V(0); }
    RAW_BAR();
    if (kt >= 1 && kt + 2 < 8) {
      int nb = buf + 2; if (nb >= 3) nb -= 3;
      stage(kt + 2, nb);
    }
    const char* base = sm + buf * K1_STAGE;
#pragma unroll
    for (int ks = 0; ks < 2; ++ks) {
      const v8s xf0 = *(const v8s*)(base + xoff[ks][0]);
      const v8s xf1 = *(const v8s*)(base + xoff[ks][1]);
      v8s af[2][3];
#pragma unroll
      for (int s = 0; s < 2; ++s)
#pragma unroll
        for (int ft = 0; ft < 3; ++ft) af[s][ft] = trfrag(base + aoff + s * 12288 + ks * 6144 + ft * 512);
#pragma unroll
      for (int s = 0; s < 2; ++s)
#pragma unroll
        for (int ft = 0; ft < 3; ++ft) {
          acc[s][ft][0] = mfma16(af[s][ft], xf0, acc[s][ft][0]);
          acc[s][ft][1] = mfma16(af[s][ft], xf1, acc[s][ft][1]);
        }
      {
        const v4u xu = __builtin_bit_cast(v4u, xf0);
        s1 = dot2(xu.x, ONE2, s1); s1 = dot2(xu.y, ONE2, s1); s1 = dot2(xu.z, ONE2, s1); s1 = dot2(xu.w, ONE2, s1);
        s2 = dot2(xu.x, xu.x, s2); s2 = dot2(xu.y, xu.y, s2); s2 = dot2(xu.z, xu.z, s2); s2 = dot2(xu.w, xu.w, s2);
      }
      if (((2 * kt + ks) & 3) == tw) {
#pragma unroll
        for (int s = 0; s < 2; ++s)
#pragma unroll
          for (int ft = 0; ft < 3; ++ft) {
            const v4u au = __builtin_bit_cast(v4u, af[s][ft]);
            float c = cs[s][ft];
            c = dot2(au.x, ONE2, c); c = dot2(au.y, ONE2, c); c = dot2(au.z, ONE2, c); c = dot2(au.w, ONE2, c);
            cs[s][ft] = c;
          }
      }
    }
    ++buf; if (buf >= 3) buf = 0;
  }

  float* st = (float*)(sm + K1_EXTRA);
  float* csm = (float*)(sm + K1_EXTRA + 1024);
  s1 += __shfl_xor(s1, 16); s1 += __shfl_xor(s1, 32);
  s2 += __shfl_xor(s2, 16); s2 += __shfl_xor(s2, 32);
#pragma unroll
  for (int s = 0; s < 2; ++s)
#pragma unroll
    for (int ft = 0; ft < 3; ++ft) {
      float c = cs[s][ft];
      c += __shfl_xor(c, 16); c += __shfl_xor(c, 32);
      cs[s][ft] = c;
    }
  if (lane < 16) {
    *(f32x2*)(st + (32 * tw + 16 * fw + lane) * 2) = (f32x2){s1, s2};
#pragma unroll
    for (int s = 0; s < 2; ++s)
#pragma unroll
      for (int ft = 0; ft < 3; ++ft) csm[(tw * 2 + s) * 96 + 48 * fw + 16 * ft + lane] = cs[s][ft];
  }
  __syncthreads();

  float mb[2], rs[2];
#pragma unroll
  for (int n = 0; n < 2; ++n) {
    const int tl = 32 * tw + 16 * (n ^ fw) + r16;
    const f32x2 sv = *(const f32x2*)(st + tl * 2);
    const float mean = sv.x * (1.f / 512.f);
    const float var = fmaxf(sv.y * (1.f / 512.f) - mean * mean, 0.f);
    const float m_b = rbf(mean);
    const float sd = rbf(sqrtf(rbf(rbf(var) + 1e-5f)));
    mb[n] = m_b; rs[n] = 1.f / sd;
    if (ct == 0 && fw == 0 && lane < 16) *(f32x2*)(stats + (size_t)(t0 + tl) * 2) = (f32x2){m_b, sd};
  }
#pragma unroll
  for (int ft = 0; ft < 3; ++ft) {
    const int fl = 48 * fw + 16 * ft + 4 * q;
    f32x4 cg = {0.f, 0.f, 0.f, 0.f}, cu = {0.f, 0.f, 0.f, 0.f};
#pragma unroll
    for (int p = 0; p < 4; ++p) {
      cg += *(const f32x4*)(csm + (p * 2 + 0) * 96 + fl);
      cu += *(const f32x4*)(csm + (p * 2 + 1) * 96 + fl);
    }
    if (f0 + fl < F_) {
#pragma unroll
      for (int n = 0; n < 2; ++n) {
        const int tl = 32 * tw + 16 * (n ^ fw) + r16;
        float hv[4];
#pragma unroll
        for (int e = 0; e < 4; ++e) {
          const float g = (acc[0][ft][n][e] - mb[n] * cg[e]) * rs[n];
          const float u = (acc[1][ft][n][e] - mb[n] * cu[e]) * rs[n];
          const unsigned gu = pk2(g, u);
          hv[e] = bflo(gu) * bfhi(gu);
        }
        v2u o = {pk2(hv[0], hv[1]), pk2(hv[2], hv[3])};
        *(v2u*)(h + (size_t)(t0 + tl) * F_ + f0 + fl) = o;
      }
    }
  }
}

constexpr int CV_CS = 4160;
constexpr int CV_TCH = 8 * CV_CS;
constexpr int CV_UOFF = 2 * CV_TCH;
constexpr int CV_URS = 160;
constexpr int K2_LDS = CV_UOFF + 80 * CV_URS;
constexpr int G2_STAGE = 16384;
constexpr int G2_NBUF = 4;
constexpr int G2_NK = 22;

template <int NA, int NB>
__device__ __forceinline__ void conv_loop(const char* tb, const char* ub, f32x4 (&acc)[2][2]) {
#pragma unroll
  for (int dl = 0; dl < 16 * (NB + 1); ++dl) {
    const v8s ta = *(const v8s*)(tb + dl * 64);
    const v8s tc = *(const v8s*)(tb + dl * 64 + 32);
    const v8s uB = *(const v8s*)(ub + (16 * NB - dl + 16) * CV_URS);
    acc[0][1] = mfma16(ta, uB, acc[0][1]);
    acc[1][1] = mfma16(tc, uB, acc[1][1]);
    if (dl < 16 * (NA + 1)) {
      const v8s uA = *(const v8s*)(ub + (16 * NA - dl + 16) * CV_URS);
      acc[0][0] = mfma16(ta, uA, acc[0][0]);
      acc[1][0] = mfma16(tc, uA, acc[1][0]);
    }
  }
}

__global__ __launch_bounds__(256, 2) void k2_conv_gemm2(const bf16* __restrict__ x, const bf16* __restrict__ ssmk,
                                                        const bf16* __restrict__ ssmD, const bf16* __restrict__ wd,
                                                        const bf16* __restrict__ h, const bf16* __restrict__ kvT,
                                                        bf16* yaT, const float* __restrict__ stats,
                                                        unsigned* cnt, bf16* __restrict__ out) {
  extern __shared__ __attribute__((aligned(16))) char sm[];
  const int tid = threadIdx.x, lane = tid & 63;
  const int w = __builtin_amdgcn_readfirstlane(tid >> 6);
  const int q = lane >> 4, r16 = lane & 15;
  const int b = blockIdx.x;

  if (b < 256) {
    const int g = b & 7, ch0 = g * 64 + 2 * (b >> 3);
    {
      const int cl = tid >> 7, i128 = tid & 127;
      const bf16* krow = ssmk + (size_t)(ch0 + cl) * S_;
      v4u ca[3], cb[3];
#pragma unroll
      for (int rr = 0; rr < 3; ++rr) {
        const int p = i128 + 128 * rr;
        const int pa = p - 4, pb = p - 3;
        ca[rr] = (v4u){0u, 0u, 0u, 0u}; cb[rr] = (v4u){0u, 0u, 0u, 0u};
        if (pa >= 0 && pa < 256) ca[rr] = *(const v4u*)(krow + pa * 8);
        if (pb >= 0 && pb < 256) cb[rr] = *(const v4u*)(krow + pb * 8);
      }
      v4u uu[2];
#pragma unroll
      for (int rr = 0; rr < 2; ++rr) uu[rr] = *(const v4u*)(kvT + (size_t)(ch0 + cl) * S_ + (i128 + 128 * rr) * 8);
      if (tid < 160) *(v4u*)(sm + CV_UOFF + tid * 16) = (v4u){0u, 0u, 0u, 0u};
#pragma unroll
      for (int rr = 0; rr < 2; ++rr) {
        const int id = i128 + 128 * rr;
        *(v4u*)(sm + CV_UOFF + ((id >> 2) + 16) * CV_URS + cl * 64 + (id & 3) * 16) = uu[rr];
      }
#pragma unroll
      for (int rr = 0; rr < 3; ++rr) {
        const int p = i128 + 128 * rr;
        if (p < 260) {
          const unsigned d[8] = {ca[rr].x, ca[rr].y, ca[rr].z, ca[rr].w, cb[rr].x, cb[rr].y, cb[rr].z, cb[rr].w};
          char* dst = sm + cl * CV_TCH + p * 16;
#pragma unroll
          for (int c = 0; c < 8; ++c) {
            const int sh = c + 1;
            v4u o;
            if ((sh & 1) == 0) {
              o = (v4u){d[sh / 2], d[sh / 2 + 1], d[sh / 2 + 2], d[sh / 2 + 3]};
            } else {
              const int m = sh / 2;
              o = (v4u){__builtin_amdgcn_alignbyte(d[m + 1], d[m], 2), __builtin_amdgcn_alignbyte(d[m + 2], d[m + 1], 2),
                        __builtin_amdgcn_alignbyte(d[m + 3], d[m + 2], 2), __builtin_amdgcn_alignbyte(d[m + 4], d[m + 3], 2)};
            }
            *(v4u*)(dst + c * CV_CS) = o;
          }
        }
      }
    }
    __syncthreads();
    const int cl = w >> 1, ro = w & 1;
    const char* tb = sm + cl * CV_TCH + (r16 & 7) * CV_CS + (q + (r16 >> 3)) * 16;
    const char* ub = sm + CV_UOFF + cl * 64 + r16 * CV_URS + q * 16;
    f32x4 acc[2][2];
#pragma unroll
    for (int i = 0; i < 2; ++i)
#pragma unroll
      for (int n = 0; n < 2; ++n) acc[i][n] = (f32x4){0.f, 0.f, 0.f, 0.f};
    if (ro == 0) conv_loop<0, 3>(tb, ub, acc); else conv_loop<1, 2>(tb, ub, acc);
    const float dd = __bfloat162float(ssmD[ch0 + cl]);
    GAS unsigned long long* yrow = (GAS unsigned long long*)(yaT + (size_t)(ch0 + cl) * S_);
#pragma unroll
    for (int n = 0; n < 2; ++n) {
      const int nt = ro == 0 ? (n ? 3 : 0) : (n ? 2 : 1);
      const int a = 16 * nt + r16;
#pragma unroll
      for (int i = 0; i < 2; ++i) {
        const v2u kr = *(const v2u*)(sm + CV_UOFF + (a + 16) * CV_URS + cl * 64 + (28 - 16 * i - 4 * q) * 2);
        const float kv0 = bfhi(kr.y), kv1 = bflo(kr.y), kv2 = bfhi(kr.x), kv3 = bflo(kr.x);
        const unsigned y01 = pk2(acc[i][n][0], acc[i][n][1]), y23 = pk2(acc[i][n][2], acc[i][n][3]);
        const unsigned e01 = pk2(kv0 * dd, kv1 * dd), e23 = pk2(kv2 * dd, kv3 * dd);
        const unsigned o01 = pk2(bflo(y01) + bflo(e01), bfhi(y01) + bfhi(e01));
        const unsigned o23 = pk2(bflo(y23) + bflo(e23), bfhi(y23) + bfhi(e23));
        const int t = 32 * a + 16 * i + 4 * q;
        __hip_atomic_store(yrow + (t >> 2), (unsigned long long)o01 | ((unsigned long long)o23 << 32),
                           __ATOMIC_RELAXED, __HIP_MEMORY_SCOPE_AGENT);
      }
    }
    WAIT_V(0);
    __syncthreads();
    if (tid == 0) __hip_atomic_fetch_add((GAS unsigned*)(cnt + g * 64), 1u, __ATOMIC_RELAXED, __HIP_MEMORY_SCOPE_AGENT);
    return;
  }

  const int c = b - 256, idx = c >> 3;
  const int rt = (c & 7) * 4 + (idx >> 3), ct = idx & 7;
  const int t0 = rt * 64, d0 = ct * 64;
  const char* hsrc0;
  const char* hsrc1;
  int wrow0, wrow1;
  const char* wsrc;
  {
    const int j0 = 2 * w, j1 = 2 * w + 1;
    const int r0 = 8 * j0 + (lane >> 3), r1 = 8 * j1 + (lane >> 3);
    hsrc0 = (const char*)h + (size_t)(t0 + r0) * (F_ * 2) + (((lane & 7) ^ ((r0 >> 1) & 7)) << 4);
    hsrc1 = (const char*)h + (size_t)(t0 + r1) * (F_ * 2) + (((lane & 7) ^ ((r1 >> 1) & 7)) << 4);
    const int dt = lane >> 4, kp8 = (lane >> 1) & 7, half = lane & 1;
    wrow0 = 16 * w + 8 * (kp8 >> 2) + (kp8 & 3);
    wrow1 = wrow0 + 4;
    wsrc = (const char*)wd + (size_t)(d0 + 16 * dt + 8 * half) * 2;
  }
  auto stage = [&](int kt, int buf) {
    char* base = sm + buf * G2_STAGE;
    glds16(hsrc0 + kt * 128, base + (2 * w) * 1024);
    glds16(hsrc1 + kt * 128, base + (2 * w + 1) * 1024);
    const int ra = min(kt * 64 + wrow0, F_ - 1), rb_ = min(kt * 64 + wrow1, F_ - 1);
    glds16(wsrc + (size_t)ra * 1024, base + 8192 + (2 * w) * 1024);
    glds16(wsrc + (size_t)rb_ * 1024, base + 8192 + (2 * w + 1) * 1024);
  };
  stage(0, 0); stage(1, 1); stage(2, 2);

  const int dw = w >> 1, tw = w & 1;
  const int aoff = 8192 + (q >> 1) * 2048 + ((((q & 1) << 2) | (r16 >> 2)) << 5) + ((lane & 3) << 3) + dw * 512;
  int boff[2];
#pragma unroll
  for (int ks = 0; ks < 2; ++ks) boff[ks] = (32 * tw + r16) * 128 + (((4 * ks + q) ^ (r16 >> 1)) << 4);
  f32x4 acc[2][2];
#pragma unroll
  for (int m = 0; m < 2; ++m)
#pragma unroll
    for (int n = 0; n < 2; ++n) acc[m][n] = (f32x4){0.f, 0.f, 0.f, 0.f};

  int buf = 0;
  for (int kt = 0; kt < G2_NK; ++kt) {
    if (kt < G2_NK - 2) { WAIT_V(8); } else if (kt == G2_NK - 2) { WAIT_V(4); } else { WAIT_V(0); }
    RAW_BAR();
    if (kt + 3 < G2_NK) stage(kt + 3, (buf + 3) & 3);
    const char* base = sm + buf * G2_STAGE;
    const int nks = (kt == G2_NK - 1) ? 1 : 2;
#pragma unroll
    for (int ks = 0; ks < 2; ++ks) {
      if (ks < nks) {
        const v8s b0 = *(const v8s*)(base + boff[ks]);
        const v8s b1 = *(const v8s*)(base + boff[ks] + 2048);
        v8s a0, a1;
        {
          const char* p = base + aoff + ks * 4096;
          v4s lo = __builtin_amdgcn_ds_read_tr16_b64_v4i16((LAS v4s*)(p));
          v4s hi = __builtin_amdgcn_ds_read_tr16_b64_v4i16((LAS v4s*)(p + 1024));
          a0 = __builtin_shufflevector(lo, hi, 0, 1, 2, 3, 4, 5, 6, 7);
          lo = __builtin_amdgcn_ds_read_tr16_b64_v4i16((LAS v4s*)(p + 256));
          hi = __builtin_amdgcn_ds_read_tr16_b64_v4i16((LAS v4s*)(p + 1024 + 256));
          a1 = __builtin_shufflevector(lo, hi, 0, 1, 2, 3, 4, 5, 6, 7);
        }
        acc[0][0] = mfma16(a0, b0, acc[0][0]);
        acc[0][1] = mfma16(a0, b1, acc[0][1]);
        acc[1][0] = mfma16(a1, b0, acc[1][0]);
        acc[1][1] = mfma16(a1, b1, acc[1][1]);
      }
    }
    buf = (buf + 1) & 3;
  }

  if (tid == 0) {
    unsigned spins = 0;
    while (__hip_atomic_load((GAS unsigned*)(cnt + ct * 64), __ATOMIC_RELAXED, __HIP_MEMORY_SCOPE_AGENT) < 32u) {
      __builtin_amdgcn_s_sleep(1);
      if (++spins > (1u << 22)) break;
    }
  }
  __syncthreads();
  {
    const __amdgpu_buffer_rsrc_t rsrc = __builtin_amdgcn_make_buffer_rsrc((void*)yaT, 0, D_ * S_ * 2, 0x00020000);
#pragma unroll
    for (int rr = 0; rr < 2; ++rr) {
      const int id = tid + 256 * rr, dl = id >> 3, ck = id & 7;
      const v4u v = __builtin_amdgcn_raw_buffer_load_b128(rsrc, (unsigned)(((d0 + dl) * S_ + t0 + ck * 8) * 2), 0, 16);
      *(v4u*)(sm + dl * 144 + ck * 16) = v;
    }
  }
  __syncthreads();
#pragma unroll
  for (int n = 0; n < 2; ++n) {
    const int t = t0 + 32 * tw + 16 * n + r16;
    const f32x2 sv = *(const f32x2*)(stats + (size_t)t * 2);
#pragma unroll
    for (int m = 0; m < 2; ++m) {
      const int dl = 32 * dw + 16 * m + 4 * q;
      const v4s yv = __builtin_amdgcn_ds_read_tr16_b64_v4i16(
          (LAS v4s*)(sm + (dl + (r16 >> 2)) * 144 + (32 * tw + 16 * n + 4 * (lane & 3)) * 2));
      const v2u yu = __builtin_bit_cast(v2u, yv);
      const v2u xv = *(const v2u*)(x + (size_t)t * D_ + d0 + dl);
      const float xs[4] = {bflo(xv.x), bfhi(xv.x), bflo(xv.y), bfhi(xv.y)};
      const float ys[4] = {bflo(yu.x), bfhi(yu.x), bflo(yu.y), bfhi(yu.y)};
      float ov[4];
#pragma unroll
      for (int e = 0; e < 4; ++e) {
        const float xn = rbf(rbf(xs[e] - sv.x) / sv.y);
        const float a = rbf(xn * ys[e]);
        ov[e] = rbf(xs[e] + a) + rbf(acc[m][n][e]);
      }
      v2u o = {pk2(ov[0], ov[1]), pk2(ov[2], ov[3])};
      *(v2u*)(out + (size_t)t * D_ + d0 + dl) = o;
    }
  }
}

extern "C" void kernel_launch(void* const* d_in, const int* in_sizes, int n_in, void* d_out, int out_size,
                              void* d_ws, size_t ws_size, hipStream_t stream) {
  const bf16* q = (const bf16*)d_in[0];
  const bf16* k = (const bf16*)d_in[1];
  const bf16* v = (const bf16*)d_in[2];
  const bf16* sk = (const bf16*)d_in[3];
  const bf16* ssk = (const bf16*)d_in[4];
  const bf16* sD = (const bf16*)d_in[5];
  const bf16* wg = (const bf16*)d_in[7];
  const bf16* wu = (const bf16*)d_in[8];
  const bf16* wd = (const bf16*)d_in[9];
  char* ws = (char*)d_ws;
  unsigned* cnt = (unsigned*)ws;
  float* stats = (float*)(ws + 4096);
  bf16* kvT = (bf16*)(ws + 65536);
  bf16* yaT = (bf16*)(ws + 65536 + (size_t)2 * 1024 * 1024);
  bf16* h = (bf16*)(ws + 65536 + (size_t)4 * 1024 * 1024);
  (void)hipFuncSetAttribute((const void*)k1_gemm1, hipFuncAttributeMaxDynamicSharedMemorySize, K1_LDS);
  (void)hipFuncSetAttribute((const void*)k2_conv_gemm2, hipFuncAttributeMaxDynamicSharedMemorySize, K2_LDS);
  k1_gemm1<<<256, 512, K1_LDS, stream>>>(q, k, v, sk, wg, wu, h, kvT, stats, cnt);
  k2_conv_gemm2<<<512, 256, K2_LDS, stream>>>(q, ssk, sD, wd, h, kvT, yaT, stats, cnt, (bf16*)d_out);
}
```

```cpp
#include <hip/hip_runtime.h>
#include <hip/hip_bf16.h>
#include <stdint.h>

typedef __hip_bfloat16 bf16;
typedef short v4s __attribute__((ext_vector_type(4)));
typedef short v8s __attribute__((ext_vector_type(8)));
typedef __bf16 v2bf __attribute__((ext_vector_type(2)));
typedef __bf16 v8bf __attribute__((ext_vector_type(8)));
typedef float f32x4 __attribute__((ext_vector_type(4)));
typedef float f32x2 __attribute__((ext_vector_type(2)));
typedef unsigned v4u __attribute__((ext_vector_type(4)));
typedef unsigned v2u __attribute__((ext_vector_type(2)));
#define LAS __attribute__((address_space(3)))
#define GAS __attribute__((address_space(1)))

constexpr int S_ = 2048, D_ = 512, F_ = 1376;
constexpr int H_RT = 43 * 4096;

__device__ __forceinline__ float bflo(unsigned w) { return __uint_as_float(w << 16); }
__device__ __forceinline__ float bfhi(unsigned w) { return __uint_as_float(w & 0xffff0000u); }
__device__ __forceinline__ f32x2 up2(unsigned w) { return (f32x2){bflo(w), bfhi(w)}; }
__device__ __forceinline__ unsigned pk2(float a, float b) {
  v2bf p = {(__bf16)a, (__bf16)b};
  return __builtin_bit_cast(unsigned, p);
}
__device__ __forceinline__ unsigned pk2v(f32x2 v) { return pk2(v.x, v.y); }
__device__ __forceinline__ f32x2 rb2(f32x2 v) { return up2(pk2v(v)); }
__device__ __forceinline__ float rbf(float a) { return bflo(pk2(a, a)); }
__device__ __forceinline__ float dot2(unsigned a, unsigned b, float c) {
  return __builtin_amdgcn_fdot2_f32_bf16(__builtin_bit_cast(v2bf, a), __builtin_bit_cast(v2bf, b), c, false);
}
__device__ __forceinline__ v8s trfrag2(const char* p0, const char* p1) {
  v4s a = __builtin_amdgcn_ds_read_tr16_b64_v4i16((LAS v4s*)(p0));
  v4s b = __builtin_amdgcn_ds_read_tr16_b64_v4i16((LAS v4s*)(p1));
  return __builtin_shufflevector(a, b, 0, 1, 2, 3, 4, 5, 6, 7);
}
__device__ __forceinline__ f32x4 mfma16(v8s a, v8s b, f32x4 c) {
  return __builtin_amdgcn_mfma_f32_16x16x32_bf16(__builtin_bit_cast(v8bf, a), __builtin_bit_cast(v8bf, b), c, 0, 0, 0);
}
__device__ __forceinline__ void dma1(unsigned voff, const void* sbase, unsigned lds) {
  unsigned keep;
  asm volatile("s_mov_b32 %0, m0\n\ts_mov_b32 m0, %3\n\ts_nop 0\n\tglobal_load_lds_dwordx4 %1, %2\n\ts_mov_b32 m0, %0"
               : "=&s"(keep) : "v"(voff), "s"(sbase), "s"(lds) : "memory");
}
__device__ __forceinline__ void dma5(unsigned va0, unsigned va1, unsigned vb0, unsigned vb1, unsigned vb2,
                                     const void* sa, const void* sb, unsigned la, unsigned lb) {
  unsigned keep;
  asm volatile("s_mov_b32 %0, m0\n\t"
               "s_mov_b32 m0, %8\n\ts_nop 0\n\tglobal_load_lds_dwordx4 %1, %6\n\t"
               "s_add_u32 m0, %8, 0x400\n\ts_nop 0\n\tglobal_load_lds_dwordx4 %2, %6\n\t"
               "s_mov_b32 m0, %9\n\ts_nop 0\n\tglobal_load_lds_dwordx4 %3, %7\n\t"
               "s_add_u32 m0, %9, 0x400\n\ts_nop 0\n\tglobal_load_lds_dwordx4 %4, %7\n\t"
               "s_add_u32 m0, %9, 0x800\n\ts_nop 0\n\tglobal_load_lds_dwordx4 %5, %7\n\t"
               "s_mov_b32 m0, %0"
               : "=&s"(keep) : "v"(va0), "v"(va1), "v"(vb0), "v"(vb1), "v"(vb2), "s"(sa), "s"(sb), "s"(la), "s"(lb)
               : "memory", "scc");
}
__device__ __forceinline__ void dma4(unsigned va0, unsigned va1, unsigned vb0, unsigned vb1,
                                     const void* sa, const void* sb, unsigned la) {
  unsigned keep;
  asm volatile("s_mov_b32 %0, m0\n\t"
               "s_mov_b32 m0, %7\n\ts_nop 0\n\tglobal_load_lds_dwordx4 %1, %5\n\t"
               "s_add_u32 m0, %7, 0x400\n\ts_nop 0\n\tglobal_load_lds_dwordx4 %2, %5\n\t"
               "s_add_u32 m0, %7, 0x2000\n\ts_nop 0\n\tglobal_load_lds_dwordx4 %3, %6\n\t"
               "s_add_u32 m0, %7, 0x2400\n\ts_nop 0\n\tglobal_load_lds_dwordx4 %4, %6\n\t"
               "s_mov_b32 m0, %0"
               : "=&s"(keep) : "v"(va0), "v"(va1), "v"(vb0), "v"(vb1), "s"(sa), "s"(sb), "s"(la)
               : "memory", "scc");
}
#define GLD32(dst, voff, sbase, imm) asm volatile("global_load_dword %0, %1, %2 offset:" #imm : "=v"(dst) : "v"(voff), "s"(sbase) : "memory")
__device__ __forceinline__ float rowsum4(float v) {
  unsigned u = __float_as_uint(v);
  auto a = __builtin_amdgcn_permlane16_swap(u, u, false, false);
  float s = __uint_as_float(a[0]) + __uint_as_float(a[1]);
  unsigned su = __float_as_uint(s);
  auto b = __builtin_amdgcn_permlane32_swap(su, su, false, false);
  return __uint_as_float(b[0]) + __uint_as_float(b[1]);
}
#define WAIT_V(n) asm volatile("s_waitcnt vmcnt(" #n ")" ::: "memory")
#define RAW_BAR() do { asm volatile("s_waitcnt lgkmcnt(0)" ::: "memory"); __builtin_amdgcn_s_barrier(); } while (0)
#define SCHED() __builtin_amdgcn_sched_barrier(0)

#define STAMP_DECL() do {} while (0)
#define STAMP_AT(i) do {} while (0)
#define STAMP_END(ptr) do {} while (0)

#define PRIO_MODE 0
constexpr int KV_RS = 136;
__device__ __forceinline__ void kv_compute(const unsigned (&kin_)[7], const unsigned (&vv)[4], v4u sk, bool first,
                                           char* lds_dst) {
  f32x2 kf[7];
#pragma unroll
  for (int i = 0; i < 7; ++i) kf[i] = up2((i < 3 && first) ? 0u : kin_[i]);
  const f32x2 wv[4] = {(f32x2){bflo(sk.x), bflo(sk.z)}, (f32x2){bfhi(sk.x), bfhi(sk.z)},
                       (f32x2){bflo(sk.y), bflo(sk.w)}, (f32x2){bfhi(sk.y), bfhi(sk.w)}};
  f32x2 r[4];
#pragma unroll
  for (int i = 0; i < 4; ++i) {
    f32x2 a = kf[i] * wv[3];
    a = __builtin_elementwise_fma(kf[i + 1], wv[2], a);
    a = __builtin_elementwise_fma(kf[i + 2], wv[1], a);
    a = __builtin_elementwise_fma(kf[i + 3], wv[0], a);
    r[i] = rb2(a) * up2(vv[i]);
  }
  *(v2u*)(lds_dst) = (v2u){pk2(r[3].x, r[2].x), pk2(r[1].x, r[0].x)};
  *(v2u*)(lds_dst + KV_RS) = (v2u){pk2(r[3].y, r[2].y), pk2(r[1].y, r[0].y)};
}
__device__ __forceinline__ void kv_store(const char* lds_tile, int tid, int b, bf16* __restrict__ kvT) {
  const int ch = tid >> 3, ck = tid & 7;
  const v2u a = *(const v2u*)(lds_tile + ch * KV_RS + ck * 16);
  const v2u c = *(const v2u*)(lds_tile + ch * KV_RS + ck * 16 + 8);
  *(v4u*)((char*)(kvT + (size_t)(b & 7) * 64 * S_ + (b >> 3) * 64) + ch * (S_ * 2) + ck * 16) = (v4u){a.x, a.y, c.x, c.y};
}

constexpr int K1_STAGE = 40960;
constexpr int K1_EXTRA = 3 * K1_STAGE;
constexpr int K1_KV = K1_EXTRA + 2048;
constexpr int K1_KIN = K1_KV + 64 * KV_RS;
constexpr int K1_LDS = K1_KIN + 17 * 1024;

__global__ __launch_bounds__(512, 2) void k1_gemm1(const bf16* __restrict__ x, const bf16* __restrict__ kin,
                                                   const bf16* __restrict__ vin, const bf16* __restrict__ shiftk,
                                                   const bf16* __restrict__ wg, const bf16* __restrict__ wu,
                                                   bf16* __restrict__ h, bf16* __restrict__ kvT,
                                                   float* __restrict__ stats, unsigned* __restrict__ cnt,
                                                   const bf16* __restrict__ wd, bf16* __restrict__ yaT) {
  extern __shared__ __attribute__((aligned(16))) char sm[];
  const int tid = threadIdx.x, lane = tid & 63;
  const int w = __builtin_amdgcn_readfirstlane(tid >> 6);
  const int b = blockIdx.x, xcd = b & 7, idx = b >> 3;
  const int rt = (xcd >> 2) * 8 + (idx & 7), ct = (xcd & 3) * 4 + (idx >> 3);
  const unsigned smb = (unsigned)(uintptr_t)(LAS char*)sm;
  STAMP_DECL();
  STAMP_AT(0);
  const int t0 = rt * 128, f0 = ct * 96;
  const bool kv_first = (b < 8) && (tid < 32);
  const unsigned kvA = (unsigned)(tid >> 5) * 4096u + (unsigned)(tid & 31) * 4u;
  const char* kbase = (const char*)kin + (size_t)(b >> 3) * 65536 + (b & 7) * 128;
  const char* vbase = (const char*)vin + (size_t)(b >> 3) * 65536 + (b & 7) * 128;
  const char* skp = (const char*)shiftk + (b & 7) * 512;
  char* kv_dst = sm + K1_KV + (2 * (tid & 31)) * KV_RS + ((tid >> 8) * 32 + 28 - 4 * ((tid >> 5) & 7)) * 2;

  if (ct >= 15) {
    unsigned k2[7], v2[4];
#pragma unroll
    for (int i = 0; i < 7; ++i) k2[i] = *(const unsigned*)(kbase + kvA + (kv_first ? i : i - 3) * 1024);
#pragma unroll
    for (int i = 0; i < 4; ++i) v2[i] = *(const unsigned*)(vbase + kvA + i * 1024);
    const v4u sk2 = *(const v4u*)(skp + (tid & 31) * 16);
    v4u xr[2][8];
#pragma unroll
    for (int ps = 0; ps < 2; ++ps)
#pragma unroll
      for (int j = 0; j < 8; ++j)
        xr[ps][j] = *(const v4u*)(x + (size_t)(t0 + 16 * w + 8 * ps + (lane >> 3)) * D_ + ((lane & 7) + 8 * j) * 8);
    if (kv_first) { k2[6] = k2[3]; k2[5] = k2[2]; k2[4] = k2[1]; k2[3] = k2[0]; }
    kv_compute(k2, v2, sk2, kv_first, kv_dst);
#pragma unroll
    for (int ps = 0; ps < 2; ++ps) {
      f32x2 sa = {0.f, 0.f};
#pragma unroll
      for (int j = 0; j < 8; ++j)
        sa += (up2(xr[ps][j].x) + up2(xr[ps][j].y)) + (up2(xr[ps][j].z) + up2(xr[ps][j].w));
      float sm1 = sa.x + sa.y;
      sm1 += __shfl_xor(sm1, 1); sm1 += __shfl_xor(sm1, 2); sm1 += __shfl_xor(sm1, 4);
      const float mean = sm1 * (1.f / 512.f);
      const f32x2 m2 = {mean, mean};
      f32x2 qq = {0.f, 0.f};
#pragma unroll
      for (int j = 0; j < 8; ++j) {
        const unsigned u4[4] = {xr[ps][j].x, xr[ps][j].y, xr[ps][j].z, xr[ps][j].w};
#pragma unroll
        for (int e = 0; e < 4; ++e) { const f32x2 dlt = up2(u4[e]) - m2; qq = __builtin_elementwise_fma(dlt, dlt, qq); }
      }
      float sq = qq.x + qq.y;
      sq += __shfl_xor(sq, 1); sq += __shfl_xor(sq, 2); sq += __shfl_xor(sq, 4);
      const float var = sq * (1.f / 512.f);
      const float sd = rbf(sqrtf(rbf(rbf(var) + 1e-5f)));
      if ((lane & 7) == 0) *(f32x2*)(stats + (size_t)(t0 + 16 * w + 8 * ps + (lane >> 3)) * 2) = (f32x2){rbf(mean), 1.f / sd};
    }
    __syncthreads();
    kv_store(sm + K1_KV, tid, b, kvT);
    *(v4u*)((char*)yaT + (size_t)b * 8192 + tid * 16) = (v4u){~0u, ~0u, ~0u, ~0u};
    if (tid < 344) { const unsigned wv = *(const volatile unsigned*)((const char*)wd + (size_t)idx * 44032 + tid * 128); asm volatile("" :: "v"(wv)); }
    STAMP_AT(1);
    STAMP_END(stp);
    return;
  }

  const unsigned vx0 = (unsigned)(lane >> 3) * 1024u + (unsigned)(((lane & 7) ^ (lane >> 4)) << 4);
  const unsigned vx1 = (vx0 ^ 64u) + 8192u;
  const unsigned k16 = ((lane >> 1) & 3) | ((lane & 8)) | ((lane & 16) >> 2);
  const unsigned vw0 = k16 * (F_ * 2) + (unsigned)(lane >> 5) * 32u + (unsigned)(lane & 1) * 16u;
  const unsigned fpstep = (ct == 14) ? 0u : 64u;
  const unsigned vw1 = vw0 + fpstep, vw2 = vw1 + fpstep;
  const char* sx = (const char*)x + (size_t)(t0 + 16 * w) * 1024;
  const char* sw = (const char*)((w >> 2) ? wu : wg) + (size_t)(16 * (w & 3)) * (F_ * 2) + f0 * 2;
  const unsigned lx = smb + w * 2048, lw = smb + 16384 + w * 3072;
  auto stage = [&](int kt, int buf) {
    dma5(vx0, vx1, vw0, vw1, vw2, sx + kt * 128, sw + (size_t)kt * (64 * F_ * 2), lx + buf * K1_STAGE, lw + buf * K1_STAGE);
  };
  stage(0, 0);
  __builtin_amdgcn_s_barrier();
  stage(1, 1);
  v4u sk;
  unsigned warm;

  const int tw = w & 3, fw = w >> 2;
  const int q = lane >> 4, r16 = lane & 15;
  int aoff[3];
#pragma unroll
  for (int sl = 0; sl < 3; ++sl) {
    int ft = sl + tw; ft = ft >= 3 ? ft - 3 : ft; ft = ft >= 3 ? ft - 3 : ft;
    aoff[sl] = 16384 + (q >> 1) * 3072 + ((((q & 1) << 2) | (r16 >> 2)) << 5) + ((lane & 3) << 3) + fw * 1536 + ft * 512;
  }
  int xoff[2][2];
#pragma unroll
  for (int ks = 0; ks < 2; ++ks)
#pragma unroll
    for (int n = 0; n < 2; ++n)
      xoff[ks][n] = (32 * tw + 16 * (n ^ fw) + r16) * 128 + (((4 * ks + q) ^ (r16 >> 1)) << 4);

  f32x4 acc[2][3][2];
#pragma unroll
  for (int s = 0; s < 2; ++s)
#pragma unroll
    for (int ft = 0; ft < 3; ++ft)
#pragma unroll
      for (int n = 0; n < 2; ++n) acc[s][ft][n] = (f32x4){0.f, 0.f, 0.f, 0.f};
  f32x4 rsum = {0.f, 0.f, 0.f, 0.f}, csum0 = {0.f, 0.f, 0.f, 0.f}, csum1 = {0.f, 0.f, 0.f, 0.f};
  const v8s ones8 = {0x3f80, 0x3f80, 0x3f80, 0x3f80, 0x3f80, 0x3f80, 0x3f80, 0x3f80};
  v8s xf[2][2];
  v4s ah[2][2][3][2];

  auto half = [&](int cur, int nxt, const char* base, bool domm, bool dold, int ktdma) {
#pragma unroll
    for (int i = 0; i < 12; ++i) {
      const int s = i / 6, sl = (i % 6) >> 1, n = i & 1;
      if (domm) {
        const v8s a8 = __builtin_shufflevector(ah[cur][s][sl][0], ah[cur][s][sl][1], 0, 1, 2, 3, 4, 5, 6, 7);
        acc[s][sl][n] = mfma16(a8, xf[cur][n], acc[s][sl][n]);
      }
      if (dold) {
        if (i == 0) {
          xf[nxt][0] = *(const v8s*)(base + xoff[nxt][0]);
          xf[nxt][1] = *(const v8s*)(base + xoff[nxt][1]);
        } else if (i <= 6) {
          const int j = i - 1, s2 = j / 3, sl2 = j % 3;
          const char* p = base + aoff[sl2] + s2 * 12288 + nxt * 6144;
          ah[nxt][s2][sl2][0] = __builtin_amdgcn_ds_read_tr16_b64_v4i16((LAS v4s*)(p));
          ah[nxt][s2][sl2][1] = __builtin_amdgcn_ds_read_tr16_b64_v4i16((LAS v4s*)(p + 256));
        }
      }
      if (domm && i >= 9) {
        if (i == 9) rsum = mfma16(ones8, xf[cur][0], rsum);
        if (i == 10) csum0 = mfma16(__builtin_shufflevector(ah[cur][0][0][0], ah[cur][0][0][1], 0, 1, 2, 3, 4, 5, 6, 7), ones8, csum0);
        if (i == 11) csum1 = mfma16(__builtin_shufflevector(ah[cur][1][0][0], ah[cur][1][0][1], 0, 1, 2, 3, 4, 5, 6, 7), ones8, csum1);
      }
      if (i == 7 && ktdma >= 0) stage(ktdma, ktdma % 3);
      SCHED();
    }
  };

#pragma unroll
  for (int kt = 0; kt < 8; ++kt) {
    if (kt == 0) { WAIT_V(5); } else if (kt < 3) { WAIT_V(8); } else if (kt < 7) { WAIT_V(5); } else { WAIT_V(0); }
    RAW_BAR();
    STAMP_AT(2 + kt);
    const char* base = sm + (kt % 3) * K1_STAGE;
    half(1, 0, base, kt > 0, true, (kt + 2 < 8) ? kt + 2 : -1);
    if (kt == 0) {
      const unsigned vr = (unsigned)(lane >> 3) * 1024u + (unsigned)(lane & 7) * 16u;
      dma1(vr, kbase + w * 8192, smb + K1_KIN + (w + 1) * 1024);
      dma1(vr, vbase + w * 8192, smb + K1_KIN + (9 + w) * 1024);
      if (w == 0 && b >= 8) dma1(vr, kbase - 8192, smb + K1_KIN);
      const unsigned so = (unsigned)(tid & 31) * 16u;
      asm volatile("global_load_dwordx4 %0, %1, %2" : "=v"(sk) : "v"(so), "s"(skp) : "memory");
      SCHED();
    }
    if (kt == 3) {
      asm volatile("" : "+v"(sk) :: "memory");
      unsigned kk[7], vv[4];
      const char* kp = sm + K1_KIN + (tid >> 5) * 512 + (tid & 31) * 4;
#pragma unroll
      for (int i = 0; i < 7; ++i) kk[i] = *(const unsigned*)(kp + (5 + i) * 128);
#pragma unroll
      for (int i = 0; i < 4; ++i) vv[i] = *(const unsigned*)(kp + 9216 + i * 128);
      kv_compute(kk, vv, sk, kv_first, kv_dst);
      SCHED();
    }
    if (kt == 4) { kv_store(sm + K1_KV, tid, b, kvT); SCHED(); }
    if (kt == 5) { *(v4u*)((char*)yaT + (size_t)b * 8192 + tid * 16) = (v4u){~0u, ~0u, ~0u, ~0u}; SCHED(); }
    if (kt == 6) {
      const unsigned wo = (unsigned)min(tid, 343) * 128u;
      asm volatile("global_load_dword %0, %1, %2" : "=v"(warm) : "v"(wo), "s"((const char*)wd + (size_t)idx * 44032) : "memory");
      SCHED();
    }
    half(0, 1, base, true, true, -1);
  }
  asm volatile("" :: "v"(warm));
  half(1, 0, sm, true, false, -1);
  STAMP_AT(10);

  float* csf = (float*)(sm + K1_EXTRA);
  float* mbf = (float*)(sm + K1_EXTRA + 1024);
  if (lane < 16) mbf[32 * tw + 16 * fw + lane] = -rbf(rsum[0] * (1.f / 512.f));
  if (r16 == 0 && tw < 3) {
    *(f32x4*)(csf + 48 * fw + 16 * tw + 4 * q) = csum0;
    *(f32x4*)(csf + 96 + 48 * fw + 16 * tw + 4 * q) = csum1;
  }
  RAW_BAR();
  STAMP_AT(11);

  {
    float nmb[2];
#pragma unroll
    for (int n = 0; n < 2; ++n) nmb[n] = mbf[32 * tw + 16 * (n ^ fw) + r16];
    char* hb = (char*)h + (size_t)(rt * 2 + (tw >> 1)) * H_RT + (q >> 1) * 256 + r16 * 16 + (q & 1) * 8;
#pragma unroll
    for (int sl = 0; sl < 3; ++sl) {
      int ft = sl + tw; ft = ft >= 3 ? ft - 3 : ft; ft = ft >= 3 ? ft - 3 : ft;
      const int fl = 48 * fw + 16 * ft;
      const f32x4 cg = *(const f32x4*)(csf + fl + 4 * q), cu = *(const f32x4*)(csf + 96 + fl + 4 * q);
      const int g16 = 6 * ct + 3 * fw + ft;
      if (g16 < 86) {
        char* hp = hb + (g16 >> 1) * 4096 + (g16 & 1) * 512;
#pragma unroll
        for (int n = 0; n < 2; ++n) {
          const f32x4 m4 = {nmb[n], nmb[n], nmb[n], nmb[n]};
          const f32x4 g = __builtin_elementwise_fma(m4, cg, acc[0][sl][n]);
          const f32x4 u = __builtin_elementwise_fma(m4, cu, acc[1][sl][n]);
          const f32x4 hv = g * u;
          *(v2u*)(hp + (((tw & 1) * 2 + (n ^ fw)) * 1024)) = (v2u){pk2(hv[0], hv[1]), pk2(hv[2], hv[3])};
        }
      }
    }
  }
  STAMP_AT(12);
  STAMP_AT(13);
  STAMP_AT(14);
  STAMP_END(stp);
}

constexpr int CV_CS = 4160;
constexpr int CV_TCH = 8 * CV_CS;
constexpr int CV_UOFF = 2 * CV_TCH;
constexpr int CV_URS = 160;
constexpr int K2_LDS = CV_UOFF + 80 * CV_URS;
#define G2RD 4
constexpr int G2_RD = G2RD;

constexpr int CV_LA = 6;
template <int NA, int NB>
__device__ __forceinline__ void conv_loop(const char* tb, const char* ub, f32x4 (&acc)[2][2]) {
  constexpr int ND = 16 * (NB + 1), NDA = 16 * (NA + 1), R = CV_LA + 1;
  v8s T0[R], T1[R], UA[R], UB[R];
#pragma unroll
  for (int dl = -CV_LA; dl < ND; ++dl) {
    const int ld = dl + CV_LA;
    if (dl >= 0) acc[0][1] = mfma16(T0[dl % R], UB[dl % R], acc[0][1]);
    if (ld < ND) {
      T0[ld % R] = *(const v8s*)(tb + ld * 64);
      UB[ld % R] = *(const v8s*)(ub + (16 * NB - ld + 16) * CV_URS);
    }
    SCHED();
    if (dl >= 0) acc[1][1] = mfma16(T1[dl % R], UB[dl % R], acc[1][1]);
    if (ld < ND) T1[ld % R] = *(const v8s*)(tb + ld * 64 + 32);
    SCHED();
    if (dl >= 0 && dl < NDA) acc[0][0] = mfma16(T0[dl % R], UA[dl % R], acc[0][0]);
    if (ld < NDA) UA[ld % R] = *(const v8s*)(ub + (16 * NA - ld + 16) * CV_URS);
    if (dl >= 0 && dl < NDA) acc[1][0] = mfma16(T1[dl % R], UA[dl % R], acc[1][0]);
    SCHED();
  }
}

__global__ __launch_bounds__(256, 2) void k2_conv_gemm2(const bf16* __restrict__ x, const bf16* __restrict__ ssmk,
                                                        const bf16* __restrict__ ssmD, const bf16* __restrict__ wd,
                                                        const bf16* __restrict__ h, const bf16* __restrict__ kvT,
                                                        bf16* yaT, const float* __restrict__ stats,
                                                        unsigned* cnt, bf16* __restrict__ out) {
  extern __shared__ __attribute__((aligned(16))) char sm[];
  const int tid = threadIdx.x, lane = tid & 63;
  const int w = __builtin_amdgcn_readfirstlane(tid >> 6);
  const int q = lane >> 4, r16 = lane & 15;
  const int b = blockIdx.x;
  const unsigned smb = (unsigned)(uintptr_t)(LAS char*)sm;
  STAMP_DECL();
  STAMP_AT(0);

  if (b < 256) {
    const int g = b & 7, ch0 = g * 64 + 2 * (b >> 3);
    const int cl = w >> 1, ro = w & 1;
    {
      const int c2 = tid >> 7, i128 = tid & 127;
      const char* krow = (const char*)(ssmk + (size_t)(ch0 + c2) * S_) + i128 * 16;
      v4u ca[2], cb[2], uu[2];
#pragma unroll
      for (int rr = 0; rr < 2; ++rr) {
        cb[rr] = *(const v4u*)(krow + rr * 2048);
        ca[rr] = *(const v4u*)(krow + rr * 2048 - ((rr == 0 && i128 == 0) ? 0 : 16));
      }
#pragma unroll
      for (int rr = 0; rr < 2; ++rr) uu[rr] = *(const v4u*)((const char*)(kvT + (size_t)(ch0 + c2) * S_) + i128 * 16 + rr * 2048);
      if (tid < 160) *(v4u*)(sm + CV_UOFF + tid * 16) = (v4u){0u, 0u, 0u, 0u};
      else if (tid < 208) { const int z = tid - 160; *(v4u*)(sm + (z / 3) * CV_CS + (z % 3) * 16) = (v4u){0u, 0u, 0u, 0u}; }
      if (i128 == 0) ca[0] = (v4u){0u, 0u, 0u, 0u};
      char* dst0 = sm + c2 * CV_TCH + (3 + i128) * 16;
#pragma unroll
      for (int rr = 0; rr < 2; ++rr) {
        const unsigned d[8] = {ca[rr].x, ca[rr].y, ca[rr].z, ca[rr].w, cb[rr].x, cb[rr].y, cb[rr].z, cb[rr].w};
        unsigned e[7];
#pragma unroll
        for (int m = 0; m < 7; ++m) e[m] = __builtin_amdgcn_alignbyte(d[m + 1], d[m], 2);
        char* dst = dst0 + rr * 2048;
        *(v4u*)(dst + 0 * CV_CS) = (v4u){e[0], e[1], e[2], e[3]};
        *(v4u*)(dst + 1 * CV_CS) = (v4u){d[1], d[2], d[3], d[4]};
        *(v4u*)(dst + 2 * CV_CS) = (v4u){e[1], e[2], e[3], e[4]};
        *(v4u*)(dst + 3 * CV_CS) = (v4u){d[2], d[3], d[4], d[5]};
        *(v4u*)(dst + 4 * CV_CS) = (v4u){e[2], e[3], e[4], e[5]};
        *(v4u*)(dst + 5 * CV_CS) = (v4u){d[3], d[4], d[5], d[6]};
        *(v4u*)(dst + 6 * CV_CS) = (v4u){e[3], e[4], e[5], e[6]};
        *(v4u*)(dst + 7 * CV_CS) = (v4u){d[4], d[5], d[6], d[7]};
      }
      char* up = sm + CV_UOFF + ((i128 >> 2) + 16) * CV_URS + c2 * 64 + (i128 & 3) * 16;
      *(v4u*)(up) = uu[0];
      *(v4u*)(up + 32 * CV_URS) = uu[1];
    }
    const float dd = __bfloat162float(ssmD[ch0 + cl]);
    STAMP_AT(1);
    RAW_BAR();
    STAMP_AT(2);
    const char* tb = sm + cl * CV_TCH + (r16 & 7) * CV_CS + (q + (r16 >> 3)) * 16;
    const char* ub = sm + CV_UOFF + cl * 64 + r16 * CV_URS + q * 16;
    f32x4 acc[2][2];
#pragma unroll
    for (int i = 0; i < 2; ++i)
#pragma unroll
      for (int n = 0; n < 2; ++n) acc[i][n] = (f32x4){0.f, 0.f, 0.f, 0.f};
    if (ro == 0) conv_loop<0, 3>(tb, ub, acc); else conv_loop<1, 2>(tb, ub, acc);
    STAMP_AT(3);
    {
      const f32x2 d2 = {dd, dd};
      const char* kvp = sm + CV_UOFF + (r16 + 16) * CV_URS + cl * 64 + (28 - 4 * q) * 2;
      GAS char* yrow = (GAS char*)(yaT + (size_t)(ch0 + cl) * S_) + r16 * 64 + q * 8;
#pragma unroll
      for (int n = 0; n < 2; ++n) {
        const int nt = ro == 0 ? (n ? 3 : 0) : (n ? 2 : 1);
#pragma unroll
        for (int i = 0; i < 2; ++i) {
          const v2u kr = *(const v2u*)(kvp + nt * (16 * CV_URS) - 32 * i);
          const f32x2 e10 = rb2(up2(kr.y) * d2), e32 = rb2(up2(kr.x) * d2);
          const f32x2 y01 = rb2((f32x2){acc[i][n][0], acc[i][n][1]}), y23 = rb2((f32x2){acc[i][n][2], acc[i][n][3]});
          const unsigned o01 = pk2(y01.x + e10.y, y01.y + e10.x);
          const unsigned o23 = pk2(y23.x + e32.y, y23.y + e32.x);
          __hip_atomic_store((GAS unsigned long long*)(yrow + nt * 1024 + i * 32),
                             (unsigned long long)o01 | ((unsigned long long)o23 << 32), __ATOMIC_RELAXED, __HIP_MEMORY_SCOPE_AGENT);
        }
      }
    }
    STAMP_AT(4);
    STAMP_END(stp);
    return;
  }

  const int c = b - 256, idx = c >> 3;
  const int rt = (c & 7) * 4 + (idx >> 3), ct = idx & 7;
  const int t0 = rt * 64, d0 = ct * 64;
  f32x2 svv;
  v2u xvv[4];
  {
    const char* xp = (const char*)x + (size_t)(t0 + 16 * w + r16) * 1024 + (d0 + 4 * q) * 2;
    svv = *(const f32x2*)((const char*)stats + (size_t)(t0 + 16 * w + r16) * 8);
#pragma unroll
    for (int m = 0; m < 4; ++m) xvv[m] = *(const v2u*)(xp + m * 32);
  }
  const unsigned vd0 = (unsigned)((lane & 8) + ((lane >> 1) & 3)) * 1024u + (unsigned)(lane >> 4) * 32u + (unsigned)(lane & 1) * 16u;
  const unsigned vd1 = vd0 + 4096u, vd2 = vd0 + 16384u, vd3 = vd0 + 20480u;
  const unsigned vl = (unsigned)lane * 16u;
  const unsigned vh0 = vl + (unsigned)(w << 10), vh1 = vl + (unsigned)(((w + 1) & 3) << 10);
  const unsigned vh2 = vl + (unsigned)(((w + 2) & 3) << 10), vh3 = vl + (unsigned)(((w + 3) & 3) << 10);
  const char* sh_ = (const char*)h + (size_t)rt * H_RT + w * 4096;
  const char* sd_ = (const char*)wd + (size_t)w * 32768 + d0 * 2;
  const unsigned lring = smb + w * (G2_RD * 4096);
  char* ring = sm + w * (G2_RD * 4096);
  const int nst = (w == 3) ? 10 : 11;
  v8s hb[G2_RD][4];
  auto issue = [&](int i) {
    const int ii = (i < nst) ? i : 0;
    const char* sd = sd_ + (size_t)ii * 131072;
    const unsigned l = lring + (i % G2_RD) * 4096;
    unsigned keep;
    asm volatile("s_mov_b32 %0, m0\n\t"
                 "s_mov_b32 m0, %6\n\ts_nop 0\n\tglobal_load_lds_dwordx4 %1, %5\n\t"
                 "s_add_u32 m0, %6, 0x400\n\ts_nop 0\n\tglobal_load_lds_dwordx4 %2, %5\n\t"
                 "s_add_u32 m0, %6, 0x800\n\ts_nop 0\n\tglobal_load_lds_dwordx4 %3, %5\n\t"
                 "s_add_u32 m0, %6, 0xc00\n\ts_nop 0\n\tglobal_load_lds_dwordx4 %4, %5\n\t"
                 "s_mov_b32 m0, %0"
                 : "=&s"(keep) : "v"(vd0), "v"(vd1), "v"(vd2), "v"(vd3), "s"(sd), "s"(l) : "memory", "scc");
    const char* sh = sh_ + (size_t)ii * 16384;
    asm volatile("global_load_dwordx4 %0, %4, %8\n\tglobal_load_dwordx4 %1, %5, %8\n\t"
                 "global_load_dwordx4 %2, %6, %8\n\tglobal_load_dwordx4 %3, %7, %8"
                 : "=&v"(hb[i % G2_RD][0]), "=&v"(hb[i % G2_RD][1]), "=&v"(hb[i % G2_RD][2]), "=&v"(hb[i % G2_RD][3])
                 : "v"(vh0), "v"(vh1), "v"(vh2), "v"(vh3), "s"(sh) : "memory");
  };
  STAMP_AT(10);
  issue(0);
  STAMP_AT(11);
  __builtin_amdgcn_s_barrier();
  STAMP_AT(12);
#pragma unroll
  for (int i = 1; i < G2_RD - 1; ++i) issue(i);
  STAMP_AT(13);

  const int aoff = (q >> 1) * 2048 + ((((q & 1) << 2) | (r16 >> 2)) << 5) + ((lane & 3) << 3);
  f32x4 acc[4][4];
#pragma unroll
  for (int m = 0; m < 4; ++m)
#pragma unroll
    for (int n = 0; n < 4; ++n) acc[m][n] = (f32x4){0.f, 0.f, 0.f, 0.f};
  v8s fa[4];
  v2u xnn[4];

#pragma unroll
  for (int i = 0; i < 11; ++i) {
    {
      constexpr int RD = G2_RD;
      const int rem = 10 - i;
      const int yn = rem < RD - 2 ? rem : RD - 2;
      if (yn >= 3) { WAIT_V(24); } else if (yn == 2) { WAIT_V(16); } else if (yn == 1) { WAIT_V(8); } else { WAIT_V(0); }
      asm volatile("" : "+v"(hb[i % RD][0]), "+v"(hb[i % RD][1]), "+v"(hb[i % RD][2]), "+v"(hb[i % RD][3]) :: "memory");
    }
    if (i == 0) STAMP_AT(1);
    if (i == 1) STAMP_AT(2);
    if (i == 3) STAMP_AT(3);
    if (i == 6) STAMP_AT(4);
    const char* base = ring + (i % G2_RD) * 4096 + aoff;
#pragma unroll
    for (int m = 0; m < 4; ++m) fa[m] = trfrag2(base + m * 256, base + m * 256 + 1024);
    if (i + G2_RD - 1 < 11) issue(i + G2_RD - 1);
    if (i == 1) {
      const f32x2 m2 = {svv.x, svv.x}, r2 = {svv.y, svv.y};
#pragma unroll
      for (int m = 0; m < 4; ++m)
        xnn[m] = (v2u){pk2v(rb2(up2(xvv[m].x) - m2) * r2), pk2v(rb2(up2(xvv[m].y) - m2) * r2)};
    }
    if (i < 10 || w != 3) {
#pragma unroll
      for (int m = 0; m < 4; ++m)
#pragma unroll
        for (int n = 0; n < 4; ++n) acc[m][n] = mfma16(fa[m], hb[i % G2_RD][n], acc[m][n]);
    }
#pragma unroll
    for (int m = 0; m < 4; ++m) asm volatile("" : "+v"(acc[m][0]), "+v"(acc[m][1]), "+v"(acc[m][2]), "+v"(acc[m][3]));
    SCHED();
  }
  STAMP_AT(5);
  const __amdgpu_buffer_rsrc_t yrsrc = __builtin_amdgcn_make_buffer_rsrc((void*)yaT, 0, D_ * S_ * 2, 0x00020000);
  const unsigned yo = (unsigned)(((d0 + (lane >> 1)) * S_ + t0 + 16 * w + (lane & 1) * 8) * 2);
  v4u yv0 = __builtin_amdgcn_raw_buffer_load_b128(yrsrc, yo, 0, 16);
  v4u yv1 = __builtin_amdgcn_raw_buffer_load_b128(yrsrc, yo, 32 * S_ * 2, 16);
  RAW_BAR();
#pragma unroll
  for (int j = 1; j < 4; ++j) {
    char* dst = sm + (((w + j) & 3) * 3 + (j - 1)) * 4096 + lane * 16;
#pragma unroll
    for (int m = 0; m < 4; ++m) *(f32x4*)(dst + m * 1024) = acc[m][j];
  }
  RAW_BAR();
  STAMP_AT(6);
  v2u ob[4];
  {
    const float rs2 = svv.y * svv.y;
    const f32x2 r2 = {rs2, rs2};
    const char* rp = sm + (w * 3) * 4096 + lane * 16;
#pragma unroll
    for (int m = 0; m < 4; ++m) {
      f32x4 o = acc[m][0];
      o += *(const f32x4*)(rp + m * 1024);
      o += *(const f32x4*)(rp + 4096 + m * 1024);
      o += *(const f32x4*)(rp + 8192 + m * 1024);
      ob[m] = (v2u){pk2v((f32x2){o[0], o[1]} * r2), pk2v((f32x2){o[2], o[3]} * r2)};
    }
  }
  {
    unsigned spins = 0;
    while (true) {
      const bool ok = yv0.x != ~0u && yv0.y != ~0u && yv0.z != ~0u && yv0.w != ~0u &&
                      yv1.x != ~0u && yv1.y != ~0u && yv1.z != ~0u && yv1.w != ~0u;
      if (__all(ok) || ++spins > (1u << 16)) break;
      __builtin_amdgcn_s_sleep(2);
      yv0 = __builtin_amdgcn_raw_buffer_load_b128(yrsrc, yo, 0, 16);
      yv1 = __builtin_amdgcn_raw_buffer_load_b128(yrsrc, yo, 32 * S_ * 2, 16);
    }
  }
  STAMP_AT(7);
  {
    char* yw = sm + 49152 + w * 3072 + (lane >> 1) * 48 + (lane & 1) * 16;
    *(v4u*)(yw) = yv0;
    *(v4u*)(yw + 32 * 48) = yv1;
    const char* yp = sm + 49152 + w * 3072 + (4 * q + (r16 >> 2)) * 48 + (lane & 3) * 8;
    char* op = (char*)out + (size_t)(t0 + 16 * w + r16) * 1024 + (d0 + 4 * q) * 2;
    asm volatile("s_waitcnt lgkmcnt(0)" ::: "memory");
#pragma unroll
    for (int m = 0; m < 4; ++m) {
      const v4s yv = __builtin_amdgcn_ds_read_tr16_b64_v4i16((LAS v4s*)(yp + m * (16 * 48)));
      const v2u yu = __builtin_bit_cast(v2u, yv);
      const f32x2 a0 = rb2(up2(xvv[m].x) + rb2(up2(xnn[m].x) * up2(yu.x)));
      const f32x2 a1 = rb2(up2(xvv[m].y) + rb2(up2(xnn[m].y) * up2(yu.y)));
      *(v2u*)(op + m * 32) = (v2u){pk2v(a0 + up2(ob[m].x)), pk2v(a1 + up2(ob[m].y))};
    }
  }
  STAMP_AT(8);
  STAMP_AT(9);
  STAMP_END(stp);
}

extern "C" void kernel_launch(void* const* d_in, const int* in_sizes, int n_in, void* d_out, int out_size,
                              void* d_ws, size_t ws_size, hipStream_t stream) {
  const bf16* q = (const bf16*)d_in[0];
  const bf16* k = (const bf16*)d_in[1];
  const bf16* v = (const bf16*)d_in[2];
  const bf16* sk = (const bf16*)d_in[3];
  const bf16* ssk = (const bf16*)d_in[4];
  const bf16* sD = (const bf16*)d_in[5];
  const bf16* wg = (const bf16*)d_in[7];
  const bf16* wu = (const bf16*)d_in[8];
  const bf16* wd = (const bf16*)d_in[9];
  char* ws = (char*)d_ws;
  unsigned* cnt = (unsigned*)ws;
  float* stats = (float*)(ws + 4096);
  bf16* kvT = (bf16*)(ws + 65536);
  bf16* yaT = (bf16*)(ws + 65536 + (size_t)2 * 1024 * 1024);
  bf16* h = (bf16*)(ws + 65536 + (size_t)4 * 1024 * 1024);
  (void)hipFuncSetAttribute((const void*)k1_gemm1, hipFuncAttributeMaxDynamicSharedMemorySize, K1_LDS);
  (void)hipFuncSetAttribute((const void*)k2_conv_gemm2, hipFuncAttributeMaxDynamicSharedMemorySize, K2_LDS);
  k1_gemm1<<<256, 512, K1_LDS, stream>>>(q, k, v, sk, wg, wu, h, kvT, stats, cnt, wd, yaT);
  k2_conv_gemm2<<<512, 256, K2_LDS, stream>>>(q, ssk, sD, wd, h, kvT, yaT, stats, cnt, (bf16*)d_out);
}
```

```cpp
#include <hip/hip_runtime.h>
#include <hip/hip_bf16.h>
#include <stdint.h>

typedef __hip_bfloat16 bf16;
typedef short v4s __attribute__((ext_vector_type(4)));
typedef short v8s __attribute__((ext_vector_type(8)));
typedef __bf16 v2bf __attribute__((ext_vector_type(2)));
typedef __bf16 v8bf __attribute__((ext_vector_type(8)));
typedef float f32x4 __attribute__((ext_vector_type(4)));
typedef float f32x2 __attribute__((ext_vector_type(2)));
typedef unsigned v4u __attribute__((ext_vector_type(4)));
typedef unsigned v2u __attribute__((ext_vector_type(2)));
#define LAS __attribute__((address_space(3)))
#define GAS __attribute__((address_space(1)))

constexpr int S_ = 2048, D_ = 512, F_ = 1376;
constexpr int H_RT = 43 * 4096;

__device__ __forceinline__ float bflo(unsigned w) { return __uint_as_float(w << 16); }
__device__ __forceinline__ float bfhi(unsigned w) { return __uint_as_float(w & 0xffff0000u); }
__device__ __forceinline__ f32x2 up2(unsigned w) { return (f32x2){bflo(w), bfhi(w)}; }
__device__ __forceinline__ unsigned pk2(float a, float b) {
  v2bf p = {(__bf16)a, (__bf16)b};
  return __builtin_bit_cast(unsigned, p);
}
__device__ __forceinline__ unsigned pk2v(f32x2 v) { return pk2(v.x, v.y); }
__device__ __forceinline__ f32x2 rb2(f32x2 v) { return up2(pk2v(v)); }
__device__ __forceinline__ float rbf(float a) { return bflo(pk2(a, a)); }
__device__ __forceinline__ float dot2(unsigned a, unsigned b, float c) {
  return __builtin_amdgcn_fdot2_f32_bf16(__builtin_bit_cast(v2bf, a), __builtin_bit_cast(v2bf, b), c, false);
}
__device__ __forceinline__ v8s trfrag2(const char* p0, const char* p1) {
  v4s a = __builtin_amdgcn_ds_read_tr16_b64_v4i16((LAS v4s*)(p0));
  v4s b = __builtin_amdgcn_ds_read_tr16_b64_v4i16((LAS v4s*)(p1));
  return __builtin_shufflevector(a, b, 0, 1, 2, 3, 4, 5, 6, 7);
}
__device__ __forceinline__ f32x4 mfma16(v8s a, v8s b, f32x4 c) {
  return __builtin_amdgcn_mfma_f32_16x16x32_bf16(__builtin_bit_cast(v8bf, a), __builtin_bit_cast(v8bf, b), c, 0, 0, 0);
}
__device__ __forceinline__ void dma1(unsigned voff, const void* sbase, unsigned lds) {
  unsigned keep;
  asm volatile("s_mov_b32 %0, m0\n\ts_mov_b32 m0, %3\n\ts_nop 0\n\tglobal_load_lds_dwordx4 %1, %2\n\ts_mov_b32 m0, %0"
               : "=&s"(keep) : "v"(voff), "s"(sbase), "s"(lds) : "memory");
}
__device__ __forceinline__ void dma5(unsigned va0, unsigned va1, unsigned vb0, unsigned vb1, unsigned vb2,
                                     const void* sa, const void* sb, unsigned la, unsigned lb) {
  unsigned keep;
  asm volatile("s_mov_b32 %0, m0\n\t"
               "s_mov_b32 m0, %8\n\ts_nop 0\n\tglobal_load_lds_dwordx4 %1, %6\n\t"
               "s_add_u32 m0, %8, 0x400\n\ts_nop 0\n\tglobal_load_lds_dwordx4 %2, %6\n\t"
               "s_mov_b32 m0, %9\n\ts_nop 0\n\tglobal_load_lds_dwordx4 %3, %7\n\t"
               "s_add_u32 m0, %9, 0x400\n\ts_nop 0\n\tglobal_load_lds_dwordx4 %4, %7\n\t"
               "s_add_u32 m0, %9, 0x800\n\ts_nop 0\n\tglobal_load_lds_dwordx4 %5, %7\n\t"
               "s_mov_b32 m0, %0"
               : "=&s"(keep) : "v"(va0), "v"(va1), "v"(vb0), "v"(vb1), "v"(vb2), "s"(sa), "s"(sb), "s"(la), "s"(lb)
               : "memory", "scc");
}
__device__ __forceinline__ void dma4(unsigned va0, unsigned va1, unsigned vb0, unsigned vb1,
                                     const void* sa, const void* sb, unsigned la) {
  unsigned keep;
  asm volatile("s_mov_b32 %0, m0\n\t"
               "s_mov_b32 m0, %7\n\ts_nop 0\n\tglobal_load_lds_dwordx4 %1, %5\n\t"
               "s_add_u32 m0, %7, 0x400\n\ts_nop 0\n\tglobal_load_lds_dwordx4 %2, %5\n\t"
               "s_add_u32 m0, %7, 0x2000\n\ts_nop 0\n\tglobal_load_lds_dwordx4 %3, %6\n\t"
               "s_add_u32 m0, %7, 0x2400\n\ts_nop 0\n\tglobal_load_lds_dwordx4 %4, %6\n\t"
               "s_mov_b32 m0, %0"
               : "=&s"(keep) : "v"(va0), "v"(va1), "v"(vb0), "v"(vb1), "s"(sa), "s"(sb), "s"(la)
               : "memory", "scc");
}
#define ST_MODE 1
__device__ __forceinline__ void gst128(void* p, v4u v) {
  asm volatile("global_store_dwordx4 %0, %1, off sc1\n\ts_nop 1" :: "v"(p), "v"(v) : "memory");
}
__device__ __forceinline__ void gst64(void* p, v2u v) {
  asm volatile("global_store_dwordx2 %0, %1, off sc1" :: "v"(p), "v"(v) : "memory");
}
#define GLD32(dst, voff, sbase, imm) asm volatile("global_load_dword %0, %1, %2 offset:" #imm : "=v"(dst) : "v"(voff), "s"(sbase) : "memory")
__device__ __forceinline__ float rowsum4(float v) {
  unsigned u = __float_as_uint(v);
  auto a = __builtin_amdgcn_permlane16_swap(u, u, false, false);
  float s = __uint_as_float(a[0]) + __uint_as_float(a[1]);
  unsigned su = __float_as_uint(s);
  auto b = __builtin_amdgcn_permlane32_swap(su, su, false, false);
  return __uint_as_float(b[0]) + __uint_as_float(b[1]);
}
#define WAIT_V(n) asm volatile("s_waitcnt vmcnt(" #n ")" ::: "memory")
#define RAW_BAR() do { asm volatile("s_waitcnt lgkmcnt(0)" ::: "memory"); __builtin_amdgcn_s_barrier(); } while (0)
#define SCHED() __builtin_amdgcn_sched_barrier(0)

#define STAMP_DECL() do {} while (0)
#define STAMP_AT(i) do {} while (0)
#define STAMP_END(ptr) do {} while (0)

#define PRIO_MODE 0
constexpr int KV_RS = 136;
__device__ __forceinline__ void kv_compute(const unsigned (&kin_)[7], const unsigned (&vv)[4], v4u sk, bool first,
                                           char* lds_dst) {
  f32x2 kf[7];
#pragma unroll
  for (int i = 0; i < 7; ++i) kf[i] = up2((i < 3 && first) ? 0u : kin_[i]);
  const f32x2 wv[4] = {(f32x2){bflo(sk.x), bflo(sk.z)}, (f32x2){bfhi(sk.x), bfhi(sk.z)},
                       (f32x2){bflo(sk.y), bflo(sk.w)}, (f32x2){bfhi(sk.y), bfhi(sk.w)}};
  f32x2 r[4];
#pragma unroll
  for (int i = 0; i < 4; ++i) {
    f32x2 a = kf[i] * wv[3];
    a = __builtin_elementwise_fma(kf[i + 1], wv[2], a);
    a = __builtin_elementwise_fma(kf[i + 2], wv[1], a);
    a = __builtin_elementwise_fma(kf[i + 3], wv[0], a);
    r[i] = rb2(a) * up2(vv[i]);
  }
  *(v2u*)(lds_dst) = (v2u){pk2(r[3].x, r[2].x), pk2(r[1].x, r[0].x)};
  *(v2u*)(lds_dst + KV_RS) = (v2u){pk2(r[3].y, r[2].y), pk2(r[1].y, r[0].y)};
}
__device__ __forceinline__ void kv_store(const char* lds_tile, int tid, int b, bf16* __restrict__ kvT) {
  const int ch = tid >> 3, ck = tid & 7;
  const v2u a = *(const v2u*)(lds_tile + ch * KV_RS + ck * 16);
  const v2u c = *(const v2u*)(lds_tile + ch * KV_RS + ck * 16 + 8);
  *(v4u*)((char*)(kvT + (size_t)(b & 7) * 64 * S_ + (b >> 3) * 64) + ch * (S_ * 2) + ck * 16) = (v4u){a.x, a.y, c.x, c.y};
}

constexpr int K1_STAGE = 40960;
constexpr int K1_EXTRA = 3 * K1_STAGE;
constexpr int K1_KV = K1_EXTRA + 2048;
constexpr int K1_KIN = K1_KV + 64 * KV_RS;
constexpr int K1_LDS = K1_KIN + 17 * 1024;

__global__ __launch_bounds__(512, 2) void k1_gemm1(const bf16* __restrict__ x, const bf16* __restrict__ kin,
                                                   const bf16* __restrict__ vin, const bf16* __restrict__ shiftk,
                                                   const bf16* __restrict__ wg, const bf16* __restrict__ wu,
                                                   bf16* __restrict__ h, bf16* __restrict__ kvT,
                                                   float* __restrict__ stats, unsigned* __restrict__ cnt,
                                                   const bf16* __restrict__ wd, bf16* __restrict__ yaT, bf16* __restrict__ wdf,
                                                   const bf16* __restrict__ ssmk) {
  extern __shared__ __attribute__((aligned(16))) char sm[];
  const int tid = threadIdx.x, lane = tid & 63;
  const int w = __builtin_amdgcn_readfirstlane(tid >> 6);
  const int b = blockIdx.x, xcd = b & 7, idx = b >> 3;
  const int rt = (xcd >> 2) * 8 + (idx & 7), ct = (xcd & 3) * 4 + (idx >> 3);
  const unsigned smb = (unsigned)(uintptr_t)(LAS char*)sm;
  STAMP_DECL();
  STAMP_AT(0);
  const int t0 = rt * 128, f0 = ct * 96;
  const bool kv_first = (b < 8) && (tid < 32);
  const unsigned kvA = (unsigned)(tid >> 5) * 4096u + (unsigned)(tid & 31) * 4u;
  const char* kbase = (const char*)kin + (size_t)(b >> 3) * 65536 + (b & 7) * 128;
  const char* vbase = (const char*)vin + (size_t)(b >> 3) * 65536 + (b & 7) * 128;
  const char* skp = (const char*)shiftk + (b & 7) * 512;
  char* kv_dst = sm + K1_KV + (2 * (tid & 31)) * KV_RS + ((tid >> 8) * 32 + 28 - 4 * ((tid >> 5) & 7)) * 2;

  const char* warmp;
  {
    const int gidx = idx * 512 + tid;
    if (gidx < 8192) {
      warmp = (const char*)x + (size_t)((xcd >> 2) * 1024 + (gidx >> 3)) * 1024 + (gidx & 7) * 128;
    } else {
      const int j = gidx - 8192, kr_ = (j >> 3) & 511, seg = j & 7;
      const int off = min((xcd & 3) * 768 + seg * 128, F_ * 2 - 4);
      warmp = (const char*)((j >> 12) ? wu : wg) + (size_t)kr_ * (F_ * 2) + off;
    }
  }
  if (ct >= 15) {
    const int gw = rt * 8 + w;
    {
      const unsigned vd0 = (unsigned)((lane & 8) + ((lane >> 1) & 3)) * 1024u + (unsigned)(lane >> 4) * 32u + (unsigned)(lane & 1) * 16u;
#pragma unroll
      for (int j = 0; j < 3; ++j) {
        const int tl = min(gw + 128 * j, 343);
        const char* src = (const char*)wd + (size_t)(tl >> 3) * 32768 + (tl & 7) * 128;
        const unsigned l = smb + (w * 3 + j) * 4096;
        dma1(vd0, src, l); dma1(vd0, src + 4096, l + 1024); dma1(vd0, src + 16384, l + 2048); dma1(vd0, src + 20480, l + 3072);
      }
    }
    unsigned k2[7], v2[4];
#pragma unroll
    for (int i = 0; i < 7; ++i) k2[i] = *(const unsigned*)(kbase + kvA + (kv_first ? i : i - 3) * 1024);
#pragma unroll
    for (int i = 0; i < 4; ++i) v2[i] = *(const unsigned*)(vbase + kvA + i * 1024);
    const v4u sk2 = *(const v4u*)(skp + (tid & 31) * 16);
    v4u xr[2][8];
#pragma unroll
    for (int ps = 0; ps < 2; ++ps)
#pragma unroll
      for (int j = 0; j < 8; ++j)
        xr[ps][j] = *(const v4u*)(x + (size_t)(t0 + 16 * w + 8 * ps + (lane >> 3)) * D_ + ((lane & 7) + 8 * j) * 8);
    if (kv_first) { k2[6] = k2[3]; k2[5] = k2[2]; k2[4] = k2[1]; k2[3] = k2[0]; }
    kv_compute(k2, v2, sk2, kv_first, kv_dst);
#pragma unroll
    for (int ps = 0; ps < 2; ++ps) {
      f32x2 sa = {0.f, 0.f};
#pragma unroll
      for (int j = 0; j < 8; ++j)
        sa += (up2(xr[ps][j].x) + up2(xr[ps][j].y)) + (up2(xr[ps][j].z) + up2(xr[ps][j].w));
      float sm1 = sa.x + sa.y;
      sm1 += __shfl_xor(sm1, 1); sm1 += __shfl_xor(sm1, 2); sm1 += __shfl_xor(sm1, 4);
      const float mean = sm1 * (1.f / 512.f);
      const f32x2 m2 = {mean, mean};
      f32x2 qq = {0.f, 0.f};
#pragma unroll
      for (int j = 0; j < 8; ++j) {
        const unsigned u4[4] = {xr[ps][j].x, xr[ps][j].y, xr[ps][j].z, xr[ps][j].w};
#pragma unroll
        for (int e = 0; e < 4; ++e) { const f32x2 dlt = up2(u4[e]) - m2; qq = __builtin_elementwise_fma(dlt, dlt, qq); }
      }
      float sq = qq.x + qq.y;
      sq += __shfl_xor(sq, 1); sq += __shfl_xor(sq, 2); sq += __shfl_xor(sq, 4);
      const float var = sq * (1.f / 512.f);
      const float sd = rbf(sqrtf(rbf(rbf(var) + 1e-5f)));
      if ((lane & 7) == 0) *(f32x2*)(stats + (size_t)(t0 + 16 * w + 8 * ps + (lane >> 3)) * 2) = (f32x2){rbf(mean), 1.f / sd};
    }
    __syncthreads();
    kv_store(sm + K1_KV, tid, b, kvT);
    gst128((char*)yaT + (size_t)b * 8192 + tid * 16, (v4u){~0u, ~0u, ~0u, ~0u});
    if (tid < 64) { const unsigned pv = *(const volatile unsigned*)((const char*)ssmk + (size_t)xcd * 262144 + idx * 8192 + tid * 128); asm volatile("" :: "v"(pv)); }
    WAIT_V(0);
    {
      const int q_ = lane >> 4, r_ = lane & 15;
      const char* base = sm + w * 12288 + (q_ >> 1) * 2048 + ((((q_ & 1) << 2) | (r_ >> 2)) << 5) + ((lane & 3) << 3);
#pragma unroll
      for (int j = 0; j < 3; ++j) {
        const int tl = gw + 128 * j;
        if (tl < 344) {
          char* dst = (char*)wdf + (size_t)((tl & 7) * 43 + (tl >> 3)) * 4096 + lane * 16;
#pragma unroll
          for (int m = 0; m < 4; ++m) gst128(dst + m * 1024, __builtin_bit_cast(v4u, trfrag2(base + j * 4096 + m * 256, base + j * 4096 + m * 256 + 1024)));
        }
      }
    }
    STAMP_AT(1);
    STAMP_END(stp);
    return;
  }

  const unsigned vx0 = (unsigned)(lane >> 3) * 1024u + (unsigned)(((lane & 7) ^ (lane >> 4)) << 4);
  const unsigned vx1 = (vx0 ^ 64u) + 8192u;
  const unsigned k16 = ((lane >> 1) & 3) | ((lane & 8)) | ((lane & 16) >> 2);
  const unsigned vw0 = k16 * (F_ * 2) + (unsigned)(lane >> 5) * 32u + (unsigned)(lane & 1) * 16u;
  const unsigned fpstep = (ct == 14) ? 0u : 64u;
  const unsigned vw1 = vw0 + fpstep, vw2 = vw1 + fpstep;
  const char* sx = (const char*)x + (size_t)(t0 + 16 * w) * 1024;
  const char* sw = (const char*)((w >> 2) ? wu : wg) + (size_t)(16 * (w & 3)) * (F_ * 2) + f0 * 2;
  const unsigned lx = smb + w * 2048, lw = smb + 16384 + w * 3072;
  const int rot1 = 0;
  auto stage = [&](int kt0, int buf) {
    const int kt = (kt0 + rot1) & 7;
    dma5(vx0, vx1, vw0, vw1, vw2, sx + kt * 128, sw + (size_t)kt * (64 * F_ * 2), lx + buf * K1_STAGE, lw + buf * K1_STAGE);
  };
  v4u sk;
  unsigned pfv;
  stage(0, 0);
  __builtin_amdgcn_s_barrier();
  stage(1, 1);
  const int tw = w & 3, fw = w >> 2;
  const int q = lane >> 4, r16 = lane & 15;
  int aoff[3];
#pragma unroll
  for (int sl = 0; sl < 3; ++sl) {
    int ft = sl + tw; ft = ft >= 3 ? ft - 3 : ft; ft = ft >= 3 ? ft - 3 : ft;
    aoff[sl] = 16384 + (q >> 1) * 3072 + ((((q & 1) << 2) | (r16 >> 2)) << 5) + ((lane & 3) << 3) + fw * 1536 + ft * 512;
  }
  int xoff[2][2];
#pragma unroll
  for (int ks = 0; ks < 2; ++ks)
#pragma unroll
    for (int n = 0; n < 2; ++n)
      xoff[ks][n] = (32 * tw + 16 * (n ^ fw) + r16) * 128 + (((4 * ks + q) ^ (r16 >> 1)) << 4);

  f32x4 acc[2][3][2];
#pragma unroll
  for (int s = 0; s < 2; ++s)
#pragma unroll
    for (int ft = 0; ft < 3; ++ft)
#pragma unroll
      for (int n = 0; n < 2; ++n) acc[s][ft][n] = (f32x4){0.f, 0.f, 0.f, 0.f};
  f32x4 rsum = {0.f, 0.f, 0.f, 0.f}, csum0 = {0.f, 0.f, 0.f, 0.f}, csum1 = {0.f, 0.f, 0.f, 0.f};
  const v8s ones8 = {0x3f80, 0x3f80, 0x3f80, 0x3f80, 0x3f80, 0x3f80, 0x3f80, 0x3f80};
  v8s xf[2][2];
  v4s ah[2][2][3][2];

  unsigned kk[7], vv[4];
  f32x2 kf[7], wv[4], kr[4];
  const char* kp = sm + K1_KIN + (tid >> 5) * 512 + (tid & 31) * 4;
  auto kvop = [&](int j) {
    if (j < 0 || j > 40) return;
    if (j < 7) kk[j] = *(const unsigned*)(kp + (5 + j) * 128);
    else if (j < 11) vv[j - 7] = *(const unsigned*)(kp + 9216 + (j - 7) * 128);
    else if (j == 11) { asm volatile("" : "+v"(sk) :: "memory"); wv[0] = (f32x2){bflo(sk.x), bflo(sk.z)}; }
    else if (j == 12) wv[1] = (f32x2){bfhi(sk.x), bfhi(sk.z)};
    else if (j == 13) wv[2] = (f32x2){bflo(sk.y), bflo(sk.w)};
    else if (j == 14) wv[3] = (f32x2){bfhi(sk.y), bfhi(sk.w)};
    else if (j < 22) kf[j - 15] = up2((j - 15 < 3 && kv_first) ? 0u : kk[j - 15]);
    else if (j < 38) {
      const int t = (j - 22) >> 2, ph = (j - 22) & 3;
      if (ph == 0) { kr[t] = kf[t] * wv[3]; kr[t] = __builtin_elementwise_fma(kf[t + 1], wv[2], kr[t]); }
      else if (ph == 1) { kr[t] = __builtin_elementwise_fma(kf[t + 2], wv[1], kr[t]); kr[t] = __builtin_elementwise_fma(kf[t + 3], wv[0], kr[t]); }
      else if (ph == 2) kr[t] = rb2(kr[t]);
      else kr[t] = kr[t] * up2(vv[t]);
    }
    else if (j == 38) *(v2u*)(kv_dst) = (v2u){pk2(kr[3].x, kr[2].x), pk2(kr[1].x, kr[0].x)};
    else if (j == 39) *(v2u*)(kv_dst + KV_RS) = (v2u){pk2(kr[3].y, kr[2].y), pk2(kr[1].y, kr[0].y)};
  };
  auto half = [&](int cur, int nxt, const char* base, bool domm, bool dold, int ktdma, int kv0) {
#pragma unroll
    for (int i = 0; i < 12; ++i) {
      const int s = i / 6, sl = (i % 6) >> 1, n = i & 1;
      if (domm) {
        const v8s a8 = __builtin_shufflevector(ah[cur][s][sl][0], ah[cur][s][sl][1], 0, 1, 2, 3, 4, 5, 6, 7);
        acc[s][sl][n] = mfma16(a8, xf[cur][n], acc[s][sl][n]);
      }
      if (dold) {
        if (i == 0) {
          xf[nxt][0] = *(const v8s*)(base + xoff[nxt][0]);
          xf[nxt][1] = *(const v8s*)(base + xoff[nxt][1]);
        } else if (i <= 6) {
          const int j = i - 1, s2 = j / 3, sl2 = j % 3;
          const char* p = base + aoff[sl2] + s2 * 12288 + nxt * 6144;
          ah[nxt][s2][sl2][0] = __builtin_amdgcn_ds_read_tr16_b64_v4i16((LAS v4s*)(p));
          ah[nxt][s2][sl2][1] = __builtin_amdgcn_ds_read_tr16_b64_v4i16((LAS v4s*)(p + 256));
        }
      }
      if (domm && i >= 9) {
        if (i == 9) rsum = mfma16(ones8, xf[cur][0], rsum);
        if (i == 10) csum0 = mfma16(__builtin_shufflevector(ah[cur][0][0][0], ah[cur][0][0][1], 0, 1, 2, 3, 4, 5, 6, 7), ones8, csum0);
        if (i == 11) csum1 = mfma16(__builtin_shufflevector(ah[cur][1][0][0], ah[cur][1][0][1], 0, 1, 2, 3, 4, 5, 6, 7), ones8, csum1);
      }
      if (kv0 > -100) kvop(kv0 + i);
      if (i == 7 && ktdma >= 0) stage(ktdma, ktdma % 3);
      SCHED();
    }
  };

#pragma unroll
  for (int kt = 0; kt < 8; ++kt) {
    if (kt == 0) { WAIT_V(5); } else if (kt < 3) { WAIT_V(8); } else if (kt < 7) { WAIT_V(5); } else { WAIT_V(0); }
    RAW_BAR();
    STAMP_AT(2 + kt);
    const char* base = sm + (kt % 3) * K1_STAGE;
    half(1, 0, base, kt > 0, true, (kt + 2 < 8) ? kt + 2 : -1, (kt == 3 || kt == 4) ? (kt - 3) * 24 : -1000);
    if (kt == 0) {
      const unsigned vr = (unsigned)(lane >> 3) * 1024u + (unsigned)(lane & 7) * 16u;
      dma1(vr, kbase + w * 8192, smb + K1_KIN + (w + 1) * 1024);
      dma1(vr, vbase + w * 8192, smb + K1_KIN + (9 + w) * 1024);
      if (w == 0 && b >= 8) dma1(vr, kbase - 8192, smb + K1_KIN);
      const unsigned so = (unsigned)(tid & 31) * 16u;
      asm volatile("global_load_dwordx4 %0, %1, %2" : "=v"(sk) : "v"(so), "s"(skp) : "memory");
      SCHED();
    }
    if (kt == 5) { kv_store(sm + K1_KV, tid, b, kvT); SCHED(); }
    if (kt == 5) {
      const unsigned po = (unsigned)(tid & 63) * 128u;
      asm volatile("global_load_dword %0, %1, %2" : "=v"(pfv) : "v"(po), "s"((const char*)ssmk + (size_t)xcd * 262144 + idx * 8192) : "memory");
      SCHED();
    }
    if (kt == 6) { gst128((char*)yaT + (size_t)b * 8192 + tid * 16, (v4u){~0u, ~0u, ~0u, ~0u}); SCHED(); }
    half(0, 1, base, true, true, -1, (kt == 3 || kt == 4) ? (kt - 3) * 24 + 12 : -1000);
  }
  asm volatile("" :: "v"(pfv));
  half(1, 0, sm, true, false, -1, -1000);
  STAMP_AT(10);

  float* csf = (float*)(sm + K1_EXTRA);
  float* mbf = (float*)(sm + K1_EXTRA + 1024);
  if (lane < 16) mbf[32 * tw + 16 * fw + lane] = -rbf(rsum[0] * (1.f / 512.f));
  if (r16 == 0 && tw < 3) {
    *(f32x4*)(csf + 48 * fw + 16 * tw + 4 * q) = csum0;
    *(f32x4*)(csf + 96 + 48 * fw + 16 * tw + 4 * q) = csum1;
  }
  RAW_BAR();
  STAMP_AT(11);

  {
    float nmb[2];
#pragma unroll
    for (int n = 0; n < 2; ++n) nmb[n] = mbf[32 * tw + 16 * (n ^ fw) + r16];
    char* hb = (char*)h + (size_t)(rt * 2 + (tw >> 1)) * H_RT + (q >> 1) * 256 + r16 * 16 + (q & 1) * 8;
#pragma unroll
    for (int sl = 0; sl < 3; ++sl) {
      int ft = sl + tw; ft = ft >= 3 ? ft - 3 : ft; ft = ft >= 3 ? ft - 3 : ft;
      const int fl = 48 * fw + 16 * ft;
      const f32x4 cg = *(const f32x4*)(csf + fl + 4 * q), cu = *(const f32x4*)(csf + 96 + fl + 4 * q);
      const int g16 = 6 * ct + 3 * fw + ft;
      if (g16 < 86) {
        char* hp = hb + (g16 >> 1) * 4096 + (g16 & 1) * 512;
#pragma unroll
        for (int n = 0; n < 2; ++n) {
          const f32x4 m4 = {nmb[n], nmb[n], nmb[n], nmb[n]};
          const f32x4 g = __builtin_elementwise_fma(m4, cg, acc[0][sl][n]);
          const f32x4 u = __builtin_elementwise_fma(m4, cu, acc[1][sl][n]);
          const f32x4 hv = g * u;
          gst64(hp + (((tw & 1) * 2 + (n ^ fw)) * 1024), (v2u){pk2(hv[0], hv[1]), pk2(hv[2], hv[3])});
        }
      }
    }
  }
  STAMP_AT(12);
  STAMP_AT(13);
  STAMP_AT(14);
  STAMP_END(stp);
}

constexpr int CV_CS = 4160;
constexpr int CV_TCH = 8 * CV_CS;
constexpr int CV_UOFF = 2 * CV_TCH;
constexpr int CV_URS = 160;
constexpr int K2_LDS = CV_UOFF + 80 * CV_URS;
#define G2RD 3
constexpr int G2_RD = G2RD;

constexpr int CV_LA = 6;
template <int NA, int NB>
__device__ __forceinline__ void conv_loop(const char* tb, const char* ub, f32x4 (&acc)[2][2]) {
  constexpr int ND = 16 * (NB + 1), NDA = 16 * (NA + 1), R = CV_LA + 1;
  v8s T0[R], T1[R], UA[R], UB[R];
#pragma unroll
  for (int dl = -CV_LA; dl < ND; ++dl) {
    const int ld = dl + CV_LA;
    if (dl >= 0) acc[0][1] = mfma16(T0[dl % R], UB[dl % R], acc[0][1]);
    if (ld < ND) {
      T0[ld % R] = *(const v8s*)(tb + ld * 64);
      UB[ld % R] = *(const v8s*)(ub + (16 * NB - ld + 16) * CV_URS);
    }
    SCHED();
    if (dl >= 0) acc[1][1] = mfma16(T1[dl % R], UB[dl % R], acc[1][1]);
    if (ld < ND) T1[ld % R] = *(const v8s*)(tb + ld * 64 + 32);
    SCHED();
    if (dl >= 0 && dl < NDA) acc[0][0] = mfma16(T0[dl % R], UA[dl % R], acc[0][0]);
    if (ld < NDA) UA[ld % R] = *(const v8s*)(ub + (16 * NA - ld + 16) * CV_URS);
    if (dl >= 0 && dl < NDA) acc[1][0] = mfma16(T1[dl % R], UA[dl % R], acc[1][0]);
    SCHED();
  }
}

__global__ __launch_bounds__(256, 2) void k2_conv_gemm2(const bf16* __restrict__ x, const bf16* __restrict__ ssmk,
                                                        const bf16* __restrict__ ssmD, const bf16* __restrict__ wd,
                                                        const bf16* __restrict__ h, const bf16* __restrict__ kvT,
                                                        bf16* yaT, const float* __restrict__ stats,
                                                        unsigned* cnt, bf16* __restrict__ out) {
  extern __shared__ __attribute__((aligned(16))) char sm[];
  const int tid = threadIdx.x, lane = tid & 63;
  const int w = __builtin_amdgcn_readfirstlane(tid >> 6);
  const int q = lane >> 4, r16 = lane & 15;
  const int b = blockIdx.x;
  const unsigned smb = (unsigned)(uintptr_t)(LAS char*)sm;
  STAMP_DECL();
  STAMP_AT(0);

  if (b < 256) {
    const int g = b & 7, ch0 = g * 64 + 2 * (b >> 3);
    const int cl = w >> 1, ro = w & 1;
    {
      const int c2 = tid >> 7, i128 = tid & 127;
      const char* krow = (const char*)(ssmk + (size_t)(ch0 + c2) * S_) + i128 * 16;
      v4u ca[2], cb[2], uu[2];
#pragma unroll
      for (int rr = 0; rr < 2; ++rr) {
        cb[rr] = *(const v4u*)(krow + rr * 2048);
        ca[rr] = *(const v4u*)(krow + rr * 2048 - ((rr == 0 && i128 == 0) ? 0 : 16));
      }
#pragma unroll
      for (int rr = 0; rr < 2; ++rr) uu[rr] = *(const v4u*)((const char*)(kvT + (size_t)(ch0 + c2) * S_) + i128 * 16 + rr * 2048);
      if (tid < 160) *(v4u*)(sm + CV_UOFF + tid * 16) = (v4u){0u, 0u, 0u, 0u};
      else if (tid < 208) { const int z = tid - 160; *(v4u*)(sm + (z / 3) * CV_CS + (z % 3) * 16) = (v4u){0u, 0u, 0u, 0u}; }
      if (i128 == 0) ca[0] = (v4u){0u, 0u, 0u, 0u};
      char* dst0 = sm + c2 * CV_TCH + (3 + i128) * 16;
#pragma unroll
      for (int rr = 0; rr < 2; ++rr) {
        const unsigned d[8] = {ca[rr].x, ca[rr].y, ca[rr].z, ca[rr].w, cb[rr].x, cb[rr].y, cb[rr].z, cb[rr].w};
        unsigned e[7];
#pragma unroll
        for (int m = 0; m < 7; ++m) e[m] = __builtin_amdgcn_alignbyte(d[m + 1], d[m], 2);
        char* dst = dst0 + rr * 2048;
        *(v4u*)(dst + 0 * CV_CS) = (v4u){e[0], e[1], e[2], e[3]};
        *(v4u*)(dst + 1 * CV_CS) = (v4u){d[1], d[2], d[3], d[4]};
        *(v4u*)(dst + 2 * CV_CS) = (v4u){e[1], e[2], e[3], e[4]};
        *(v4u*)(dst + 3 * CV_CS) = (v4u){d[2], d[3], d[4], d[5]};
        *(v4u*)(dst + 4 * CV_CS) = (v4u){e[2], e[3], e[4], e[5]};
        *(v4u*)(dst + 5 * CV_CS) = (v4u){d[3], d[4], d[5], d[6]};
        *(v4u*)(dst + 6 * CV_CS) = (v4u){e[3], e[4], e[5], e[6]};
        *(v4u*)(dst + 7 * CV_CS) = (v4u){d[4], d[5], d[6], d[7]};
      }
      char* up = sm + CV_UOFF + ((i128 >> 2) + 16) * CV_URS + c2 * 64 + (i128 & 3) * 16;
      *(v4u*)(up) = uu[0];
      *(v4u*)(up + 32 * CV_URS) = uu[1];
    }
    const float dd = __bfloat162float(ssmD[ch0 + cl]);
    STAMP_AT(1);
    RAW_BAR();
    STAMP_AT(2);
    const char* tb = sm + cl * CV_TCH + (r16 & 7) * CV_CS + (q + (r16 >> 3)) * 16;
    const char* ub = sm + CV_UOFF + cl * 64 + r16 * CV_URS + q * 16;
    f32x4 acc[2][2];
#pragma unroll
    for (int i = 0; i < 2; ++i)
#pragma unroll
      for (int n = 0; n < 2; ++n) acc[i][n] = (f32x4){0.f, 0.f, 0.f, 0.f};
    if (ro == 0) conv_loop<0, 3>(tb, ub, acc); else conv_loop<1, 2>(tb, ub, acc);
    STAMP_AT(3);
    {
      const f32x2 d2 = {dd, dd};
      const char* kvp = sm + CV_UOFF + (r16 + 16) * CV_URS + cl * 64 + (28 - 4 * q) * 2;
      GAS char* yrow = (GAS char*)(yaT + (size_t)(ch0 + cl) * S_) + r16 * 64 + q * 8;
#pragma unroll
      for (int n = 0; n < 2; ++n) {
        const int nt = ro == 0 ? (n ? 3 : 0) : (n ? 2 : 1);
#pragma unroll
        for (int i = 0; i < 2; ++i) {
          const v2u kr = *(const v2u*)(kvp + nt * (16 * CV_URS) - 32 * i);
          const f32x2 e10 = rb2(up2(kr.y) * d2), e32 = rb2(up2(kr.x) * d2);
          const f32x2 y01 = rb2((f32x2){acc[i][n][0], acc[i][n][1]}), y23 = rb2((f32x2){acc[i][n][2], acc[i][n][3]});
          const unsigned o01 = pk2(y01.x + e10.y, y01.y + e10.x);
          const unsigned o23 = pk2(y23.x + e32.y, y23.y + e32.x);
          __hip_atomic_store((GAS unsigned long long*)(yrow + nt * 1024 + i * 32),
                             (unsigned long long)o01 | ((unsigned long long)o23 << 32), __ATOMIC_RELAXED, __HIP_MEMORY_SCOPE_AGENT);
        }
      }
    }
    STAMP_AT(4);
    STAMP_END(stp);
    return;
  }

  const int c = b - 256, idx = c >> 3;
  const int rt = (c & 7) * 4 + (idx >> 3), ct = idx & 7;
  const int t0 = rt * 64, d0 = ct * 64;
  const unsigned vl = (unsigned)lane * 16u;
  const unsigned vh0 = vl + (unsigned)(w << 10), vh1 = vl + (unsigned)(((w + 1) & 3) << 10);
  const unsigned vh2 = vl + (unsigned)(((w + 2) & 3) << 10), vh3 = vl + (unsigned)(((w + 3) & 3) << 10);
  const char* sh_ = (const char*)h + (size_t)rt * H_RT + w * 4096;
  const char* sd_ = (const char*)wd + (size_t)ct * H_RT + w * 4096;
  const int nst = (w == 3) ? 10 : 11;
  const int rot2 = 0;
  v8s hb[G2_RD][4], fa[G2_RD][4];
  auto issue = [&](int i) {
    int ii = ((i < nst) ? i : 0) + rot2;
    ii = ii >= nst ? ii - nst : ii;
    const char* sh = sh_ + (size_t)ii * 16384;
    const char* sd = sd_ + (size_t)ii * 16384;
    asm volatile("global_load_dwordx4 %0, %8, %13\n\tglobal_load_dwordx4 %4, %9, %14\n\t"
                 "global_load_dwordx4 %1, %8, %13 offset:1024\n\tglobal_load_dwordx4 %5, %10, %14\n\t"
                 "global_load_dwordx4 %2, %8, %13 offset:2048\n\tglobal_load_dwordx4 %6, %11, %14\n\t"
                 "global_load_dwordx4 %3, %8, %13 offset:3072\n\tglobal_load_dwordx4 %7, %12, %14"
                 : "=&v"(fa[i % G2_RD][0]), "=&v"(fa[i % G2_RD][1]), "=&v"(fa[i % G2_RD][2]), "=&v"(fa[i % G2_RD][3]),
                   "=&v"(hb[i % G2_RD][0]), "=&v"(hb[i % G2_RD][1]), "=&v"(hb[i % G2_RD][2]), "=&v"(hb[i % G2_RD][3])
                 : "v"(vl), "v"(vh0), "v"(vh1), "v"(vh2), "v"(vh3), "s"(sd), "s"(sh) : "memory");
  };
  issue(0);
  __builtin_amdgcn_s_barrier();
#pragma unroll
  for (int i = 1; i < G2_RD; ++i) issue(i);

  f32x4 acc[4][4];
#pragma unroll
  for (int m = 0; m < 4; ++m)
#pragma unroll
    for (int n = 0; n < 4; ++n) acc[m][n] = (f32x4){0.f, 0.f, 0.f, 0.f};
  v2u svu, xvv[4], xnn[4];
  v4u yv0, yv1;
  const unsigned vxo = (unsigned)r16 * 1024u + (unsigned)q * 8u;
  const char* sxp = (const char*)x + (size_t)(t0 + 16 * w) * 1024 + d0 * 2;
  const unsigned vyo = (unsigned)(lane >> 1) * (S_ * 2) + (unsigned)(lane & 1) * 16u;
  const char* syp = (const char*)yaT + (size_t)d0 * (S_ * 2) + (t0 + 16 * w) * 2;

#pragma unroll
  for (int i = 0; i < 11; ++i) {
    {
      constexpr int RD = G2_RD;
      const int rem = 10 - i;
      const int yn = rem < RD - 1 ? rem : RD - 1;
      if (yn >= 3) { WAIT_V(24); } else if (yn == 2) { WAIT_V(16); } else if (yn == 1) { WAIT_V(8); } else { WAIT_V(0); }
      asm volatile("" : "+v"(hb[i % RD][0]), "+v"(hb[i % RD][1]), "+v"(hb[i % RD][2]), "+v"(hb[i % RD][3]),
                        "+v"(fa[i % RD][0]), "+v"(fa[i % RD][1]), "+v"(fa[i % RD][2]), "+v"(fa[i % RD][3]) :: "memory");
    }
    if (i == 0) STAMP_AT(1);
    if (i == 1) STAMP_AT(2);
    if (i == 3) STAMP_AT(3);
    if (i == 6) STAMP_AT(4);
    if (i < 10 || w != 3) {
#pragma unroll
      for (int m = 0; m < 4; ++m)
#pragma unroll
        for (int n = 0; n < 4; ++n) acc[m][n] = mfma16(fa[i % G2_RD][m], hb[i % G2_RD][n], acc[m][n]);
    }
#pragma unroll
    for (int m = 0; m < 4; ++m) asm volatile("" : "+v"(acc[m][0]), "+v"(acc[m][1]), "+v"(acc[m][2]), "+v"(acc[m][3]));
    SCHED();
    if (i + G2_RD < 11) issue(i + G2_RD);
    if (i == 2) {
      asm volatile("global_load_dwordx2 %0, %5, %6\n\tglobal_load_dwordx2 %1, %5, %6 offset:32\n\t"
                   "global_load_dwordx2 %2, %5, %6 offset:64\n\tglobal_load_dwordx2 %3, %5, %6 offset:96\n\t"
                   "global_load_dwordx2 %4, %7, %8"
                   : "=&v"(xvv[0]), "=&v"(xvv[1]), "=&v"(xvv[2]), "=&v"(xvv[3]), "=&v"(svu)
                   : "v"(vxo), "s"(sxp), "v"((unsigned)r16 * 8u), "s"((const char*)stats + (size_t)(t0 + 16 * w) * 8) : "memory");
    }
    if (i == 7) {
      asm volatile("global_load_dwordx4 %0, %2, %3 sc1\n\tglobal_load_dwordx4 %1, %2, %4 sc1"
                   : "=&v"(yv0), "=&v"(yv1) : "v"(vyo), "s"(syp), "s"(syp + 32 * S_ * 2) : "memory");
    }
    if (i == 8) {
      asm volatile("" : "+v"(xvv[0]), "+v"(xvv[1]), "+v"(xvv[2]), "+v"(xvv[3]), "+v"(svu) :: "memory");
      const float mean = __uint_as_float(svu.x), rs = __uint_as_float(svu.y);
      const f32x2 m2 = {mean, mean}, r2 = {rs, rs};
#pragma unroll
      for (int m = 0; m < 4; ++m)
        xnn[m] = (v2u){pk2v(rb2(up2(xvv[m].x) - m2) * r2), pk2v(rb2(up2(xvv[m].y) - m2) * r2)};
    }
    SCHED();
  }
  STAMP_AT(5);
#pragma unroll
  for (int j = 1; j < 4; ++j) {
    char* dst = sm + (((w + j) & 3) * 3 + (j - 1)) * 4096 + lane * 16;
#pragma unroll
    for (int m = 0; m < 4; ++m) *(f32x4*)(dst + m * 1024) = acc[m][j];
  }
  asm volatile("" : "+v"(yv0), "+v"(yv1) :: "memory");
  {
    const __amdgpu_buffer_rsrc_t yrsrc = __builtin_amdgcn_make_buffer_rsrc((void*)yaT, 0, D_ * S_ * 2, 0x00020000);
    const unsigned yo = (unsigned)(d0 * (S_ * 2) + (t0 + 16 * w) * 2) + vyo;
    unsigned spins = 0;
    while (true) {
      const bool ok = yv0.x != ~0u && yv0.y != ~0u && yv0.z != ~0u && yv0.w != ~0u &&
                      yv1.x != ~0u && yv1.y != ~0u && yv1.z != ~0u && yv1.w != ~0u;
      if (__all(ok) || ++spins > (1u << 16)) break;
      __builtin_amdgcn_s_sleep(2);
      yv0 = __builtin_amdgcn_raw_buffer_load_b128(yrsrc, yo, 0, 16);
      yv1 = __builtin_amdgcn_raw_buffer_load_b128(yrsrc, yo, 32 * S_ * 2, 16);
    }
  }
  STAMP_AT(6);
  f32x2 av[4][2];
  {
    char* yw = sm + 49152 + w * 3072 + (lane >> 1) * 48 + (lane & 1) * 16;
    *(v4u*)(yw) = yv0;
    *(v4u*)(yw + 32 * 48) = yv1;
    const char* yp = sm + 49152 + w * 3072 + (4 * q + (r16 >> 2)) * 48 + (lane & 3) * 8;
    asm volatile("s_waitcnt lgkmcnt(0)" ::: "memory");
    __builtin_amdgcn_s_barrier();
#pragma unroll
    for (int m = 0; m < 4; ++m) {
      const v4s yv = __builtin_amdgcn_ds_read_tr16_b64_v4i16((LAS v4s*)(yp + m * (16 * 48)));
      const v2u yu = __builtin_bit_cast(v2u, yv);
      av[m][0] = rb2(up2(xvv[m].x) + rb2(up2(xnn[m].x) * up2(yu.x)));
      av[m][1] = rb2(up2(xvv[m].y) + rb2(up2(xnn[m].y) * up2(yu.y)));
    }
  }
  STAMP_AT(7);
  {
    const float rs = __uint_as_float(svu.y), rs2 = rs * rs;
    const f32x2 r2 = {rs2, rs2};
    const char* rp = sm + (w * 3) * 4096 + lane * 16;
    char* op = (char*)out + (size_t)(t0 + 16 * w + r16) * 1024 + (d0 + 4 * q) * 2;
#pragma unroll
    for (int m = 0; m < 4; ++m) {
      f32x4 o = acc[m][0];
      o += *(const f32x4*)(rp + m * 1024);
      o += *(const f32x4*)(rp + 4096 + m * 1024);
      o += *(const f32x4*)(rp + 8192 + m * 1024);
      *(v2u*)(op + m * 32) = (v2u){pk2v(av[m][0] + rb2((f32x2){o[0], o[1]} * r2)), pk2v(av[m][1] + rb2((f32x2){o[2], o[3]} * r2))};
    }
  }
  STAMP_AT(8);
  STAMP_AT(9);
  STAMP_END(stp);
}

extern "C" void kernel_launch(void* const* d_in, const int* in_sizes, int n_in, void* d_out, int out_size,
                              void* d_ws, size_t ws_size, hipStream_t stream) {
  const bf16* q = (const bf16*)d_in[0];
  const bf16* k = (const bf16*)d_in[1];
  const bf16* v = (const bf16*)d_in[2];
  const bf16* sk = (const bf16*)d_in[3];
  const bf16* ssk = (const bf16*)d_in[4];
  const bf16* sD = (const bf16*)d_in[5];
  const bf16* wg = (const bf16*)d_in[7];
  const bf16* wu = (const bf16*)d_in[8];
  const bf16* wd = (const bf16*)d_in[9];
  char* ws = (char*)d_ws;
  unsigned* cnt = (unsigned*)ws;
  float* stats = (float*)(ws + 4096);
  bf16* kvT = (bf16*)(ws + 65536);
  bf16* yaT = (bf16*)(ws + 65536 + (size_t)2 * 1024 * 1024);
  bf16* h = (bf16*)(ws + 65536 + (size_t)4 * 1024 * 1024);
  bf16* wdf = (bf16*)(ws + 65536 + (size_t)10 * 1024 * 1024);
  (void)hipFuncSetAttribute((const void*)k1_gemm1, hipFuncAttributeMaxDynamicSharedMemorySize, K1_LDS);
  (void)hipFuncSetAttribute((const void*)k2_conv_gemm2, hipFuncAttributeMaxDynamicSharedMemorySize, K2_LDS);
  k1_gemm1<<<256, 512, K1_LDS, stream>>>(q, k, v, sk, wg, wu, h, kvT, stats, cnt, wd, yaT, wdf, ssk);
  k2_conv_gemm2<<<512, 256, K2_LDS, stream>>>(q, ssk, sD, wdf, h, kvT, yaT, stats, cnt, (bf16*)d_out);
}
```

```cpp
#include <hip/hip_runtime.h>
#include <hip/hip_bf16.h>
#include <stdint.h>

typedef __hip_bfloat16 bf16;
typedef short v4s __attribute__((ext_vector_type(4)));
typedef short v8s __attribute__((ext_vector_type(8)));
typedef __bf16 v2bf __attribute__((ext_vector_type(2)));
typedef __bf16 v8bf __attribute__((ext_vector_type(8)));
typedef float f32x4 __attribute__((ext_vector_type(4)));
typedef float f32x2 __attribute__((ext_vector_type(2)));
typedef unsigned v4u __attribute__((ext_vector_type(4)));
typedef unsigned v2u __attribute__((ext_vector_type(2)));
#define LAS __attribute__((address_space(3)))
#define GAS __attribute__((address_space(1)))

constexpr int S_ = 2048, D_ = 512, F_ = 1376;
constexpr int H_RT = 43 * 4096;

__device__ __forceinline__ float bflo(unsigned w) { return __uint_as_float(w << 16); }
__device__ __forceinline__ float bfhi(unsigned w) { return __uint_as_float(w & 0xffff0000u); }
__device__ __forceinline__ f32x2 up2(unsigned w) { return (f32x2){bflo(w), bfhi(w)}; }
__device__ __forceinline__ unsigned pk2(float a, float b) {
  v2bf p = {(__bf16)a, (__bf16)b};
  return __builtin_bit_cast(unsigned, p);
}
__device__ __forceinline__ unsigned pk2v(f32x2 v) { return pk2(v.x, v.y); }
__device__ __forceinline__ f32x2 rb2(f32x2 v) { return up2(pk2v(v)); }
__device__ __forceinline__ float rbf(float a) { return bflo(pk2(a, a)); }
__device__ __forceinline__ float dot2(unsigned a, unsigned b, float c) {
  return __builtin_amdgcn_fdot2_f32_bf16(__builtin_bit_cast(v2bf, a), __builtin_bit_cast(v2bf, b), c, false);
}
__device__ __forceinline__ v8s trfrag2(const char* p0, const char* p1) {
  v4s a = __builtin_amdgcn_ds_read_tr16_b64_v4i16((LAS v4s*)(p0));
  v4s b = __builtin_amdgcn_ds_read_tr16_b64_v4i16((LAS v4s*)(p1));
  return __builtin_shufflevector(a, b, 0, 1, 2, 3, 4, 5, 6, 7);
}
__device__ __forceinline__ f32x4 mfma16(v8s a, v8s b, f32x4 c) {
  return __builtin_amdgcn_mfma_f32_16x16x32_bf16(__builtin_bit_cast(v8bf, a), __builtin_bit_cast(v8bf, b), c, 0, 0, 0);
}
__device__ __forceinline__ void dma1(unsigned voff, const void* sbase, unsigned lds) {
  unsigned keep;
  asm volatile("s_mov_b32 %0, m0\n\ts_mov_b32 m0, %3\n\ts_nop 0\n\tglobal_load_lds_dwordx4 %1, %2\n\ts_mov_b32 m0, %0"
               : "=&s"(keep) : "v"(voff), "s"(sbase), "s"(lds) : "memory");
}
__device__ __forceinline__ void dma5(unsigned va0, unsigned va1, unsigned vb0, unsigned vb1, unsigned vb2,
                                     const void* sa, const void* sb, unsigned la, unsigned lb) {
  unsigned keep;
  asm volatile("s_mov_b32 %0, m0\n\t"
               "s_mov_b32 m0, %8\n\ts_nop 0\n\tglobal_load_lds_dwordx4 %1, %6\n\t"
               "s_add_u32 m0, %8, 0x400\n\ts_nop 0\n\tglobal_load_lds_dwordx4 %2, %6\n\t"
               "s_mov_b32 m0, %9\n\ts_nop 0\n\tglobal_load_lds_dwordx4 %3, %7\n\t"
               "s_add_u32 m0, %9, 0x400\n\ts_nop 0\n\tglobal_load_lds_dwordx4 %4, %7\n\t"
               "s_add_u32 m0, %9, 0x800\n\ts_nop 0\n\tglobal_load_lds_dwordx4 %5, %7\n\t"
               "s_mov_b32 m0, %0"
               : "=&s"(keep) : "v"(va0), "v"(va1), "v"(vb0), "v"(vb1), "v"(vb2), "s"(sa), "s"(sb), "s"(la), "s"(lb)
               : "memory", "scc");
}
__device__ __forceinline__ void dma4(unsigned va0, unsigned va1, unsigned vb0, unsigned vb1,
                                     const void* sa, const void* sb, unsigned la) {
  unsigned keep;
  asm volatile("s_mov_b32 %0, m0\n\t"
               "s_mov_b32 m0, %7\n\ts_nop 0\n\tglobal_load_lds_dwordx4 %1, %5\n\t"
               "s_add_u32 m0, %7, 0x400\n\ts_nop 0\n\tglobal_load_lds_dwordx4 %2, %5\n\t"
               "s_add_u32 m0, %7, 0x2000\n\ts_nop 0\n\tglobal_load_lds_dwordx4 %3, %6\n\t"
               "s_add_u32 m0, %7, 0x2400\n\ts_nop 0\n\tglobal_load_lds_dwordx4 %4, %6\n\t"
               "s_mov_b32 m0, %0"
               : "=&s"(keep) : "v"(va0), "v"(va1), "v"(vb0), "v"(vb1), "s"(sa), "s"(sb), "s"(la)
               : "memory", "scc");
}
__device__ __forceinline__ void gst128(void* p, v4u v) {
  asm volatile("global_store_dwordx4 %0, %1, off sc1\n\ts_nop 1" :: "v"(p), "v"(v) : "memory");
}
__device__ __forceinline__ void gst64(void* p, v2u v) {
  asm volatile("global_store_dwordx2 %0, %1, off sc1" :: "v"(p), "v"(v) : "memory");
}
#define GLD32(dst, voff, sbase, imm) asm volatile("global_load_dword %0, %1, %2 offset:" #imm : "=v"(dst) : "v"(voff), "s"(sbase) : "memory")
__device__ __forceinline__ float rowsum4(float v) {
  unsigned u = __float_as_uint(v);
  auto a = __builtin_amdgcn_permlane16_swap(u, u, false, false);
  float s = __uint_as_float(a[0]) + __uint_as_float(a[1]);
  unsigned su = __float_as_uint(s);
  auto b = __builtin_amdgcn_permlane32_swap(su, su, false, false);
  return __uint_as_float(b[0]) + __uint_as_float(b[1]);
}
#define WAIT_V(n) asm volatile("s_waitcnt vmcnt(" #n ")" ::: "memory")
#define RAW_BAR() do { asm volatile("s_waitcnt lgkmcnt(0)" ::: "memory"); __builtin_amdgcn_s_barrier(); } while (0)
#define SCHED() __builtin_amdgcn_sched_barrier(0)

constexpr int KV_RS = 136;
__device__ __forceinline__ void kv_compute(const unsigned (&kin_)[7], const unsigned (&vv)[4], v4u sk, bool first,
                                           char* lds_dst) {
  f32x2 kf[7];
#pragma unroll
  for (int i = 0; i < 7; ++i) kf[i] = up2((i < 3 && first) ? 0u : kin_[i]);
  const f32x2 wv[4] = {(f32x2){bflo(sk.x), bflo(sk.z)}, (f32x2){bfhi(sk.x), bfhi(sk.z)},
                       (f32x2){bflo(sk.y), bflo(sk.w)}, (f32x2){bfhi(sk.y), bfhi(sk.w)}};
  f32x2 r[4];
#pragma unroll
  for (int i = 0; i < 4; ++i) {
    f32x2 a = kf[i] * wv[3];
    a = __builtin_elementwise_fma(kf[i + 1], wv[2], a);
    a = __builtin_elementwise_fma(kf[i + 2], wv[1], a);
    a = __builtin_elementwise_fma(kf[i + 3], wv[0], a);
    r[i] = rb2(a) * up2(vv[i]);
  }
  *(v2u*)(lds_dst) = (v2u){pk2(r[3].x, r[2].x), pk2(r[1].x, r[0].x)};
  *(v2u*)(lds_dst + KV_RS) = (v2u){pk2(r[3].y, r[2].y), pk2(r[1].y, r[0].y)};
}
__device__ __forceinline__ void kv_store(const char* lds_tile, int tid, int b, bf16* __restrict__ kvT) {
  const int ch = tid >> 3, ck = tid & 7;
  const v2u a = *(const v2u*)(lds_tile + ch * KV_RS + ck * 16);
  const v2u c = *(const v2u*)(lds_tile + ch * KV_RS + ck * 16 + 8);
  *(v4u*)((char*)(kvT + (size_t)(b & 7) * 64 * S_ + (b >> 3) * 64) + ch * (S_ * 2) + ck * 16) = (v4u){a.x, a.y, c.x, c.y};
}

constexpr int K1_STAGE = 40960;
constexpr int K1_EXTRA = 3 * K1_STAGE;
constexpr int K1_KV = K1_EXTRA + 2048;
constexpr int K1_KIN = K1_KV + 64 * KV_RS;
constexpr int K1_LDS = K1_KIN + 17 * 1024;

__global__ __launch_bounds__(512, 2) void k1_gemm1(const bf16* __restrict__ x, const bf16* __restrict__ kin,
                                                   const bf16* __restrict__ vin, const bf16* __restrict__ shiftk,
                                                   const bf16* __restrict__ wg, const bf16* __restrict__ wu,
                                                   bf16* __restrict__ h, bf16* __restrict__ kvT,
                                                   float* __restrict__ stats,
                                                   const bf16* __restrict__ wd, bf16* __restrict__ yaT, bf16* __restrict__ wdf,
                                                   const bf16* __restrict__ ssmk) {
  extern __shared__ __attribute__((aligned(16))) char sm[];
  const int tid = threadIdx.x, lane = tid & 63;
  const int w = __builtin_amdgcn_readfirstlane(tid >> 6);
  const int b = blockIdx.x, xcd = b & 7, idx = b >> 3;
  const int rt = (xcd >> 2) * 8 + (idx & 7), ct = (xcd & 3) * 4 + (idx >> 3);
  const unsigned smb = (unsigned)(uintptr_t)(LAS char*)sm;
  const int t0 = rt * 128, f0 = ct * 96;
  const bool kv_first = (b < 8) && (tid < 32);
  const unsigned kvA = (unsigned)(tid >> 5) * 4096u + (unsigned)(tid & 31) * 4u;
  const char* kbase = (const char*)kin + (size_t)(b >> 3) * 65536 + (b & 7) * 128;
  const char* vbase = (const char*)vin + (size_t)(b >> 3) * 65536 + (b & 7) * 128;
  const char* skp = (const char*)shiftk + (b & 7) * 512;
  char* kv_dst = sm + K1_KV + (2 * (tid & 31)) * KV_RS + ((tid >> 8) * 32 + 28 - 4 * ((tid >> 5) & 7)) * 2;

  if (ct >= 15) {
    const int gw = rt * 8 + w;
    {
      const unsigned vd0 = (unsigned)((lane & 8) + ((lane >> 1) & 3)) * 1024u + (unsigned)(lane >> 4) * 32u + (unsigned)(lane & 1) * 16u;
#pragma unroll
      for (int j = 0; j < 3; ++j) {
        const int tl = min(gw + 128 * j, 343);
        const char* src = (const char*)wd + (size_t)(tl >> 3) * 32768 + (tl & 7) * 128;
        const unsigned l = smb + (w * 3 + j) * 4096;
        dma1(vd0, src, l); dma1(vd0, src + 4096, l + 1024); dma1(vd0, src + 16384, l + 2048); dma1(vd0, src + 20480, l + 3072);
      }
    }
    unsigned k2[7], v2[4];
#pragma unroll
    for (int i = 0; i < 7; ++i) k2[i] = *(const unsigned*)(kbase + kvA + (kv_first ? i : i - 3) * 1024);
#pragma unroll
    for (int i = 0; i < 4; ++i) v2[i] = *(const unsigned*)(vbase + kvA + i * 1024);
    const v4u sk2 = *(const v4u*)(skp + (tid & 31) * 16);
    v4u xr[2][8];
#pragma unroll
    for (int ps = 0; ps < 2; ++ps)
#pragma unroll
      for (int j = 0; j < 8; ++j)
        xr[ps][j] = *(const v4u*)(x + (size_t)(t0 + 16 * w + 8 * ps + (lane >> 3)) * D_ + ((lane & 7) + 8 * j) * 8);
    if (kv_first) { k2[6] = k2[3]; k2[5] = k2[2]; k2[4] = k2[1]; k2[3] = k2[0]; }
    kv_compute(k2, v2, sk2, kv_first, kv_dst);
#pragma unroll
    for (int ps = 0; ps < 2; ++ps) {
      f32x2 sa = {0.f, 0.f};
#pragma unroll
      for (int j = 0; j < 8; ++j)
        sa += (up2(xr[ps][j].x) + up2(xr[ps][j].y)) + (up2(xr[ps][j].z) + up2(xr[ps][j].w));
      float sm1 = sa.x + sa.y;
      sm1 += __shfl_xor(sm1, 1); sm1 += __shfl_xor(sm1, 2); sm1 += __shfl_xor(sm1, 4);
      const float mean = sm1 * (1.f / 512.f);
      const f32x2 m2 = {mean, mean};
      f32x2 qq = {0.f, 0.f};
#pragma unroll
      for (int j = 0; j < 8; ++j) {
        const unsigned u4[4] = {xr[ps][j].x, xr[ps][j].y, xr[ps][j].z, xr[ps][j].w};
#pragma unroll
        for (int e = 0; e < 4; ++e) { const f32x2 dlt = up2(u4[e]) - m2; qq = __builtin_elementwise_fma(dlt, dlt, qq); }
      }
      float sq = qq.x + qq.y;
      sq += __shfl_xor(sq, 1); sq += __shfl_xor(sq, 2); sq += __shfl_xor(sq, 4);
      const float var = sq * (1.f / 512.f);
      const float sd = rbf(sqrtf(rbf(rbf(var) + 1e-5f)));
      if ((lane & 7) == 0) *(f32x2*)(stats + (size_t)(t0 + 16 * w + 8 * ps + (lane >> 3)) * 2) = (f32x2){rbf(mean), 1.f / sd};
    }
    __syncthreads();
    kv_store(sm + K1_KV, tid, b, kvT);
    gst128((char*)yaT + (size_t)b * 8192 + tid * 16, (v4u){~0u, ~0u, ~0u, ~0u});
    if (tid < 64) { const unsigned pv = *(const volatile unsigned*)((const char*)ssmk + (size_t)xcd * 262144 + idx * 8192 + tid * 128); asm volatile("" :: "v"(pv)); }
    WAIT_V(0);
    {
      const int q_ = lane >> 4, r_ = lane & 15;
      const char* base = sm + w * 12288 + (q_ >> 1) * 2048 + ((((q_ & 1) << 2) | (r_ >> 2)) << 5) + ((lane & 3) << 3);
#pragma unroll
      for (int j = 0; j < 3; ++j) {
        const int tl = gw + 128 * j;
        if (tl < 344) {
          char* dst = (char*)wdf + (size_t)((tl & 7) * 43 + (tl >> 3)) * 4096 + lane * 16;
#pragma unroll
          for (int m = 0; m < 4; ++m) gst128(dst + m * 1024, __builtin_bit_cast(v4u, trfrag2(base + j * 4096 + m * 256, base + j * 4096 + m * 256 + 1024)));
        }
      }
    }
    return;
  }

  const unsigned vx0 = (unsigned)(lane >> 3) * 1024u + (unsigned)(((lane & 7) ^ (lane >> 4)) << 4);
  const unsigned vx1 = (vx0 ^ 64u) + 8192u;
  const unsigned k16 = ((lane >> 1) & 3) | ((lane & 8)) | ((lane & 16) >> 2);
  const unsigned vw0 = k16 * (F_ * 2) + (unsigned)(lane >> 5) * 32u + (unsigned)(lane & 1) * 16u;
  const unsigned fpstep = (ct == 14) ? 0u : 64u;
  const unsigned vw1 = vw0 + fpstep, vw2 = vw1 + fpstep;
  const char* sx = (const char*)x + (size_t)(t0 + 16 * w) * 1024;
  const char* sw = (const char*)((w >> 2) ? wu : wg) + (size_t)(16 * (w & 3)) * (F_ * 2) + f0 * 2;
  const unsigned lx = smb + w * 2048, lw = smb + 16384 + w * 3072;
  auto stage = [&](int kt, int buf) {
    dma5(vx0, vx1, vw0, vw1, vw2, sx + kt * 128, sw + (size_t)kt * (64 * F_ * 2), lx + buf * K1_STAGE, lw + buf * K1_STAGE);
  };
  v4u sk;
  unsigned pfv;
  stage(0, 0);
  __builtin_amdgcn_s_barrier();
  stage(1, 1);
  const int tw = w & 3, fw = w >> 2;
  const int q = lane >> 4, r16 = lane & 15;
  int aoff[3];
#pragma unroll
  for (int sl = 0; sl < 3; ++sl) {
    int ft = sl + tw; ft = ft >= 3 ? ft - 3 : ft; ft = ft >= 3 ? ft - 3 : ft;
    aoff[sl] = 16384 + (q >> 1) * 3072 + ((((q & 1) << 2) | (r16 >> 2)) << 5) + ((lane & 3) << 3) + fw * 1536 + ft * 512;
  }
  int xoff[2][2];
#pragma unroll
  for (int ks = 0; ks < 2; ++ks)
#pragma unroll
    for (int n = 0; n < 2; ++n)
      xoff[ks][n] = (32 * tw + 16 * (n ^ fw) + r16) * 128 + (((4 * ks + q) ^ (r16 >> 1)) << 4);

  f32x4 acc[2][3][2];
#pragma unroll
  for (int s = 0; s < 2; ++s)
#pragma unroll
    for (int ft = 0; ft < 3; ++ft)
#pragma unroll
      for (int n = 0; n < 2; ++n) acc[s][ft][n] = (f32x4){0.f, 0.f, 0.f, 0.f};
  f32x4 rsum = {0.f, 0.f, 0.f, 0.f}, csum0 = {0.f, 0.f, 0.f, 0.f}, csum1 = {0.f, 0.f, 0.f, 0.f};
  const v8s ones8 = {0x3f80, 0x3f80, 0x3f80, 0x3f80, 0x3f80, 0x3f80, 0x3f80, 0x3f80};
  v8s xf[2][2];
  v4s ah[2][2][3][2];

  unsigned kk[7], vv[4];
  f32x2 kf[7], wv[4], kr[4];
  const char* kp = sm + K1_KIN + (tid >> 5) * 512 + (tid & 31) * 4;
  auto kvop = [&](int j) {
    if (j < 0 || j > 40) return;
    if (j < 7) kk[j] = *(const unsigned*)(kp + (5 + j) * 128);
    else if (j < 11) vv[j - 7] = *(const unsigned*)(kp + 9216 + (j - 7) * 128);
    else if (j == 11) { asm volatile("" : "+v"(sk) :: "memory"); wv[0] = (f32x2){bflo(sk.x), bflo(sk.z)}; }
    else if (j == 12) wv[1] = (f32x2){bfhi(sk.x), bfhi(sk.z)};
    else if (j == 13) wv[2] = (f32x2){bflo(sk.y), bflo(sk.w)};
    else if (j == 14) wv[3] = (f32x2){bfhi(sk.y), bfhi(sk.w)};
    else if (j < 22) kf[j - 15] = up2((j - 15 < 3 && kv_first) ? 0u : kk[j - 15]);
    else if (j < 38) {
      const int t = (j - 22) >> 2, ph = (j - 22) & 3;
      if (ph == 0) { kr[t] = kf[t] * wv[3]; kr[t] = __builtin_elementwise_fma(kf[t + 1], wv[2], kr[t]); }
      else if (ph == 1) { kr[t] = __builtin_elementwise_fma(kf[t + 2], wv[1], kr[t]); kr[t] = __builtin_elementwise_fma(kf[t + 3], wv[0], kr[t]); }
      else if (ph == 2) kr[t] = rb2(kr[t]);
      else kr[t] = kr[t] * up2(vv[t]);
    }
    else if (j == 38) *(v2u*)(kv_dst) = (v2u){pk2(kr[3].x, kr[2].x), pk2(kr[1].x, kr[0].x)};
    else if (j == 39) *(v2u*)(kv_dst + KV_RS) = (v2u){pk2(kr[3].y, kr[2].y), pk2(kr[1].y, kr[0].y)};
  };
  auto half = [&](int cur, int nxt, const char* base, bool domm, bool dold, int ktdma, int kv0) {
#pragma unroll
    for (int i = 0; i < 12; ++i) {
      const int s = i / 6, sl = (i % 6) >> 1, n = i & 1;
      if (domm) {
        const v8s a8 = __builtin_shufflevector(ah[cur][s][sl][0], ah[cur][s][sl][1], 0, 1, 2, 3, 4, 5, 6, 7);
        acc[s][sl][n] = mfma16(a8, xf[cur][n], acc[s][sl][n]);
      }
      if (dold) {
        if (i == 0) {
          xf[nxt][0] = *(const v8s*)(base + xoff[nxt][0]);
          xf[nxt][1] = *(const v8s*)(base + xoff[nxt][1]);
        } else if (i <= 6) {
          const int j = i - 1, s2 = j / 3, sl2 = j % 3;
          const char* p = base + aoff[sl2] + s2 * 12288 + nxt * 6144;
          ah[nxt][s2][sl2][0] = __builtin_amdgcn_ds_read_tr16_b64_v4i16((LAS v4s*)(p));
          ah[nxt][s2][sl2][1] = __builtin_amdgcn_ds_read_tr16_b64_v4i16((LAS v4s*)(p + 256));
        }
      }
      if (domm && i >= 9) {
        if (i == 9) rsum = mfma16(ones8, xf[cur][0], rsum);
        if (i == 10) csum0 = mfma16(__builtin_shufflevector(ah[cur][0][0][0], ah[cur][0][0][1], 0, 1, 2, 3, 4, 5, 6, 7), ones8, csum0);
        if (i == 11) csum1 = mfma16(__builtin_shufflevector(ah[cur][1][0][0], ah[cur][1][0][1], 0, 1, 2, 3, 4, 5, 6, 7), ones8, csum1);
      }
      if (kv0 > -100) kvop(kv0 + i);
      if (i == 7 && ktdma >= 0) stage(ktdma, ktdma % 3);
      SCHED();
    }
  };

#pragma unroll
  for (int kt = 0; kt < 8; ++kt) {
    if (kt == 0) { WAIT_V(5); } else if (kt < 3) { WAIT_V(8); } else if (kt < 7) { WAIT_V(5); } else { WAIT_V(0); }
    RAW_BAR();
    const char* base = sm + (kt % 3) * K1_STAGE;
    half(1, 0, base, kt > 0, true, (kt + 2 < 8) ? kt + 2 : -1, (kt == 3 || kt == 4) ? (kt - 3) * 24 : -1000);
    if (kt == 0) {
      const unsigned vr = (unsigned)(lane >> 3) * 1024u + (unsigned)(lane & 7) * 16u;
      dma1(vr, kbase + w * 8192, smb + K1_KIN + (w + 1) * 1024);
      dma1(vr, vbase + w * 8192, smb + K1_KIN + (9 + w) * 1024);
      if (w == 0 && b >= 8) dma1(vr, kbase - 8192, smb + K1_KIN);
      const unsigned so = (unsigned)(tid & 31) * 16u;
      asm volatile("global_load_dwordx4 %0, %1, %2" : "=v"(sk) : "v"(so), "s"(skp) : "memory");
      SCHED();
    }
    if (kt == 5) { kv_store(sm + K1_KV, tid, b, kvT); SCHED(); }
    if (kt == 5) {
      const unsigned po = (unsigned)(tid & 63) * 128u;
      asm volatile("global_load_dword %0, %1, %2" : "=v"(pfv) : "v"(po), "s"((const char*)ssmk + (size_t)xcd * 262144 + idx * 8192) : "memory");
      SCHED();
    }
    if (kt == 6) { gst128((char*)yaT + (size_t)b * 8192 + tid * 16, (v4u){~0u, ~0u, ~0u, ~0u}); SCHED(); }
    half(0, 1, base, true, true, -1, (kt == 3 || kt == 4) ? (kt - 3) * 24 + 12 : -1000);
  }
  asm volatile("" :: "v"(pfv));
  half(1, 0, sm, true, false, -1, -1000);
  float* csf = (float*)(sm + K1_EXTRA);
  float* mbf = (float*)(sm + K1_EXTRA + 1024);
  if (lane < 16) mbf[32 * tw + 16 * fw + lane] = -rbf(rsum[0] * (1.f / 512.f));
  if (r16 == 0 && tw < 3) {
    *(f32x4*)(csf + 48 * fw + 16 * tw + 4 * q) = csum0;
    *(f32x4*)(csf + 96 + 48 * fw + 16 * tw + 4 * q) = csum1;
  }
  RAW_BAR();
  {
    float nmb[2];
#pragma unroll
    for (int n = 0; n < 2; ++n) nmb[n] = mbf[32 * tw + 16 * (n ^ fw) + r16];
    char* hb = (char*)h + (size_t)(rt * 2 + (tw >> 1)) * H_RT + (q >> 1) * 256 + r16 * 16 + (q & 1) * 8;
#pragma unroll
    for (int sl = 0; sl < 3; ++sl) {
      int ft = sl + tw; ft = ft >= 3 ? ft - 3 : ft; ft = ft >= 3 ? ft - 3 : ft;
      const int fl = 48 * fw + 16 * ft;
      const f32x4 cg = *(const f32x4*)(csf + fl + 4 * q), cu = *(const f32x4*)(csf + 96 + fl + 4 * q);
      const int g16 = 6 * ct + 3 * fw + ft;
      if (g16 < 86) {
        char* hp = hb + (g16 >> 1) * 4096 + (g16 & 1) * 512;
#pragma unroll
        for (int n = 0; n < 2; ++n) {
          const f32x4 m4 = {nmb[n], nmb[n], nmb[n], nmb[n]};
          const f32x4 g = __builtin_elementwise_fma(m4, cg, acc[0][sl][n]);
          const f32x4 u = __builtin_elementwise_fma(m4, cu, acc[1][sl][n]);
          const f32x4 hv = g * u;
          gst64(hp + (((tw & 1) * 2 + (n ^ fw)) * 1024), (v2u){pk2(hv[0], hv[1]), pk2(hv[2], hv[3])});
        }
      }
    }
  }
}

constexpr int CV_CS = 4160;
constexpr int CV_TCH = 8 * CV_CS;
constexpr int CV_UOFF = 2 * CV_TCH;
constexpr int CV_URS = 160;
constexpr int K2_LDS = CV_UOFF + 80 * CV_URS;
constexpr int G2_RD = 3;

constexpr int CV_LA = 6;
template <int NA, int NB>
__device__ __forceinline__ void conv_loop(const char* tb, const char* ub, f32x4 (&acc)[2][2]) {
  constexpr int ND = 16 * (NB + 1), NDA = 16 * (NA + 1), R = CV_LA + 1;
  v8s T0[R], T1[R], UA[R], UB[R];
#pragma unroll
  for (int dl = -CV_LA; dl < ND; ++dl) {
    const int ld = dl + CV_LA;
    if (dl >= 0) acc[0][1] = mfma16(T0[dl % R], UB[dl % R], acc[0][1]);
    if (ld < ND) {
      T0[ld % R] = *(const v8s*)(tb + ld * 64);
      UB[ld % R] = *(const v8s*)(ub + (16 * NB - ld + 16) * CV_URS);
    }
    SCHED();
    if (dl >= 0) acc[1][1] = mfma16(T1[dl % R], UB[dl % R], acc[1][1]);
    if (ld < ND) T1[ld % R] = *(const v8s*)(tb + ld * 64 + 32);
    SCHED();
    if (dl >= 0 && dl < NDA) acc[0][0] = mfma16(T0[dl % R], UA[dl % R], acc[0][0]);
    if (ld < NDA) UA[ld % R] = *(const v8s*)(ub + (16 * NA - ld + 16) * CV_URS);
    if (dl >= 0 && dl < NDA) acc[1][0] = mfma16(T1[dl % R], UA[dl % R], acc[1][0]);
    SCHED();
  }
}

__global__ __launch_bounds__(256, 2) void k2_conv_gemm2(const bf16* __restrict__ x, const bf16* __restrict__ ssmk,
                                                        const bf16* __restrict__ ssmD, const bf16* __restrict__ wd,
                                                        const bf16* __restrict__ h, const bf16* __restrict__ kvT,
                                                        bf16* yaT, const float* __restrict__ stats,
                                                        bf16* __restrict__ out) {
  extern __shared__ __attribute__((aligned(16))) char sm[];
  const int tid = threadIdx.x, lane = tid & 63;
  const int w = __builtin_amdgcn_readfirstlane(tid >> 6);
  const int q = lane >> 4, r16 = lane & 15;
  const int b = blockIdx.x;
  const unsigned smb = (unsigned)(uintptr_t)(LAS char*)sm;
  if (b < 256) {
    const int g = b & 7, ch0 = g * 64 + 2 * (b >> 3);
    const int cl = w >> 1, ro = w & 1;
    {
      const int c2 = tid >> 7, i128 = tid & 127;
      const char* krow = (const char*)(ssmk + (size_t)(ch0 + c2) * S_) + i128 * 16;
      v4u ca[2], cb[2], uu[2];
#pragma unroll
      for (int rr = 0; rr < 2; ++rr) {
        cb[rr] = *(const v4u*)(krow + rr * 2048);
        ca[rr] = *(const v4u*)(krow + rr * 2048 - ((rr == 0 && i128 == 0) ? 0 : 16));
      }
#pragma unroll
      for (int rr = 0; rr < 2; ++rr) uu[rr] = *(const v4u*)((const char*)(kvT + (size_t)(ch0 + c2) * S_) + i128 * 16 + rr * 2048);
      if (tid < 160) *(v4u*)(sm + CV_UOFF + tid * 16) = (v4u){0u, 0u, 0u, 0u};
      else if (tid < 208) { const int z = tid - 160; *(v4u*)(sm + (z / 3) * CV_CS + (z % 3) * 16) = (v4u){0u, 0u, 0u, 0u}; }
      if (i128 == 0) ca[0] = (v4u){0u, 0u, 0u, 0u};
      char* dst0 = sm + c2 * CV_TCH + (3 + i128) * 16;
#pragma unroll
      for (int rr = 0; rr < 2; ++rr) {
        const unsigned d[8] = {ca[rr].x, ca[rr].y, ca[rr].z, ca[rr].w, cb[rr].x, cb[rr].y, cb[rr].z, cb[rr].w};
        unsigned e[7];
#pragma unroll
        for (int m = 0; m < 7; ++m) e[m] = __builtin_amdgcn_alignbyte(d[m + 1], d[m], 2);
        char* dst = dst0 + rr * 2048;
        *(v4u*)(dst + 0 * CV_CS) = (v4u){e[0], e[1], e[2], e[3]};
        *(v4u*)(dst + 1 * CV_CS) = (v4u){d[1], d[2], d[3], d[4]};
        *(v4u*)(dst + 2 * CV_CS) = (v4u){e[1], e[2], e[3], e[4]};
        *(v4u*)(dst + 3 * CV_CS) = (v4u){d[2], d[3], d[4], d[5]};
        *(v4u*)(dst + 4 * CV_CS) = (v4u){e[2], e[3], e[4], e[5]};
        *(v4u*)(dst + 5 * CV_CS) = (v4u){d[3], d[4], d[5], d[6]};
        *(v4u*)(dst + 6 * CV_CS) = (v4u){e[3], e[4], e[5], e[6]};
        *(v4u*)(dst + 7 * CV_CS) = (v4u){d[4], d[5], d[6], d[7]};
      }
      char* up = sm + CV_UOFF + ((i128 >> 2) + 16) * CV_URS + c2 * 64 + (i128 & 3) * 16;
      *(v4u*)(up) = uu[0];
      *(v4u*)(up + 32 * CV_URS) = uu[1];
    }
    const float dd = __bfloat162float(ssmD[ch0 + cl]);
    RAW_BAR();
    const char* tb = sm + cl * CV_TCH + (r16 & 7) * CV_CS + (q + (r16 >> 3)) * 16;
    const char* ub = sm + CV_UOFF + cl * 64 + r16 * CV_URS + q * 16;
    f32x4 acc[2][2];
#pragma unroll
    for (int i = 0; i < 2; ++i)
#pragma unroll
      for (int n = 0; n < 2; ++n) acc[i][n] = (f32x4){0.f, 0.f, 0.f, 0.f};
    if (ro == 0) conv_loop<0, 3>(tb, ub, acc); else conv_loop<1, 2>(tb, ub, acc);
    {
      const f32x2 d2 = {dd, dd};
      const char* kvp = sm + CV_UOFF + (r16 + 16) * CV_URS + cl * 64 + (28 - 4 * q) * 2;
      GAS char* yrow = (GAS char*)(yaT + (size_t)(ch0 + cl) * S_) + r16 * 64 + q * 8;
#pragma unroll
      for (int n = 0; n < 2; ++n) {
        const int nt = ro == 0 ? (n ? 3 : 0) : (n ? 2 : 1);
#pragma unroll
        for (int i = 0; i < 2; ++i) {
          const v2u kr = *(const v2u*)(kvp + nt * (16 * CV_URS) - 32 * i);
          const f32x2 e10 = rb2(up2(kr.y) * d2), e32 = rb2(up2(kr.x) * d2);
          const f32x2 y01 = rb2((f32x2){acc[i][n][0], acc[i][n][1]}), y23 = rb2((f32x2){acc[i][n][2], acc[i][n][3]});
          const unsigned o01 = pk2(y01.x + e10.y, y01.y + e10.x);
          const unsigned o23 = pk2(y23.x + e32.y, y23.y + e32.x);
          __hip_atomic_store((GAS unsigned long long*)(yrow + nt * 1024 + i * 32),
                             (unsigned long long)o01 | ((unsigned long long)o23 << 32), __ATOMIC_RELAXED, __HIP_MEMORY_SCOPE_AGENT);
        }
      }
    }
    return;
  }

  const int c = b - 256, idx = c >> 3;
  const int rt = (c & 7) * 4 + (idx >> 3), ct = idx & 7;
  const int t0 = rt * 64, d0 = ct * 64;
  const unsigned vl = (unsigned)lane * 16u;
  const unsigned vh0 = vl + (unsigned)(w << 10), vh1 = vl + (unsigned)(((w + 1) & 3) << 10);
  const unsigned vh2 = vl + (unsigned)(((w + 2) & 3) << 10), vh3 = vl + (unsigned)(((w + 3) & 3) << 10);
  const char* sh_ = (const char*)h + (size_t)rt * H_RT + w * 4096;
  const char* sd_ = (const char*)wd + (size_t)ct * H_RT + w * 4096;
  const int nst = (w == 3) ? 10 : 11;
  v8s hb[G2_RD][4], fa[G2_RD][4];
  auto issue = [&](int i) {
    const int ii = (i < nst) ? i : 0;
    const char* sh = sh_ + (size_t)ii * 16384;
    const char* sd = sd_ + (size_t)ii * 16384;
    asm volatile("global_load_dwordx4 %0, %8, %13\n\tglobal_load_dwordx4 %4, %9, %14\n\t"
                 "global_load_dwordx4 %1, %8, %13 offset:1024\n\tglobal_load_dwordx4 %5, %10, %14\n\t"
                 "global_load_dwordx4 %2, %8, %13 offset:2048\n\tglobal_load_dwordx4 %6, %11, %14\n\t"
                 "global_load_dwordx4 %3, %8, %13 offset:3072\n\tglobal_load_dwordx4 %7, %12, %14"
                 : "=&v"(fa[i % G2_RD][0]), "=&v"(fa[i % G2_RD][1]), "=&v"(fa[i % G2_RD][2]), "=&v"(fa[i % G2_RD][3]),
                   "=&v"(hb[i % G2_RD][0]), "=&v"(hb[i % G2_RD][1]), "=&v"(hb[i % G2_RD][2]), "=&v"(hb[i % G2_RD][3])
                 : "v"(vl), "v"(vh0), "v"(vh1), "v"(vh2), "v"(vh3), "s"(sd), "s"(sh) : "memory");
  };
  issue(0);
  __builtin_amdgcn_s_barrier();
#pragma unroll
  for (int i = 1; i < G2_RD; ++i) issue(i);

  f32x4 acc[4][4];
#pragma unroll
  for (int m = 0; m < 4; ++m)
#pragma unroll
    for (int n = 0; n < 4; ++n) acc[m][n] = (f32x4){0.f, 0.f, 0.f, 0.f};
  v2u svu, xvv[4], xnn[4];
  v4u yv0, yv1;
  const unsigned vxo = (unsigned)r16 * 1024u + (unsigned)q * 8u;
  const char* sxp = (const char*)x + (size_t)(t0 + 16 * w) * 1024 + d0 * 2;
  const unsigned vyo = (unsigned)(lane >> 1) * (S_ * 2) + (unsigned)(lane & 1) * 16u;
  const char* syp = (const char*)yaT + (size_t)d0 * (S_ * 2) + (t0 + 16 * w) * 2;

#pragma unroll
  for (int i = 0; i < 11; ++i) {
    {
      constexpr int RD = G2_RD;
      const int rem = 10 - i;
      const int yn = rem < RD - 1 ? rem : RD - 1;
      if (yn >= 3) { WAIT_V(24); } else if (yn == 2) { WAIT_V(16); } else if (yn == 1) { WAIT_V(8); } else { WAIT_V(0); }
      asm volatile("" : "+v"(hb[i % RD][0]), "+v"(hb[i % RD][1]), "+v"(hb[i % RD][2]), "+v"(hb[i % RD][3]),
                        "+v"(fa[i % RD][0]), "+v"(fa[i % RD][1]), "+v"(fa[i % RD][2]), "+v"(fa[i % RD][3]) :: "memory");
    }
    if (i < 10 || w != 3) {
#pragma unroll
      for (int m = 0; m < 4; ++m)
#pragma unroll
        for (int n = 0; n < 4; ++n) acc[m][n] = mfma16(fa[i % G2_RD][m], hb[i % G2_RD][n], acc[m][n]);
    }
#pragma unroll
    for (int m = 0; m < 4; ++m) asm volatile("" : "+v"(acc[m][0]), "+v"(acc[m][1]), "+v"(acc[m][2]), "+v"(acc[m][3]));
    SCHED();
    if (i + G2_RD < 11) issue(i + G2_RD);
    if (i == 2) {
      asm volatile("global_load_dwordx2 %0, %5, %6\n\tglobal_load_dwordx2 %1, %5, %6 offset:32\n\t"
                   "global_load_dwordx2 %2, %5, %6 offset:64\n\tglobal_load_dwordx2 %3, %5, %6 offset:96\n\t"
                   "global_load_dwordx2 %4, %7, %8"
                   : "=&v"(xvv[0]), "=&v"(xvv[1]), "=&v"(xvv[2]), "=&v"(xvv[3]), "=&v"(svu)
                   : "v"(vxo), "s"(sxp), "v"((unsigned)r16 * 8u), "s"((const char*)stats + (size_t)(t0 + 16 * w) * 8) : "memory");
    }
    if (i == 7) {
      asm volatile("global_load_dwordx4 %0, %2, %3 sc1\n\tglobal_load_dwordx4 %1, %2, %4 sc1"
                   : "=&v"(yv0), "=&v"(yv1) : "v"(vyo), "s"(syp), "s"(syp + 32 * S_ * 2) : "memory");
    }
    if (i == 8) {
      asm volatile("" : "+v"(xvv[0]), "+v"(xvv[1]), "+v"(xvv[2]), "+v"(xvv[3]), "+v"(svu) :: "memory");
      const float mean = __uint_as_float(svu.x), rs = __uint_as_float(svu.y);
      const f32x2 m2 = {mean, mean}, r2 = {rs, rs};
#pragma unroll
      for (int m = 0; m < 4; ++m)
        xnn[m] = (v2u){pk2v(rb2(up2(xvv[m].x) - m2) * r2), pk2v(rb2(up2(xvv[m].y) - m2) * r2)};
    }
    SCHED();
  }
#pragma unroll
  for (int j = 1; j < 4; ++j) {
    char* dst = sm + (((w + j) & 3) * 3 + (j - 1)) * 4096 + lane * 16;
#pragma unroll
    for (int m = 0; m < 4; ++m) *(f32x4*)(dst + m * 1024) = acc[m][j];
  }
  asm volatile("" : "+v"(yv0), "+v"(yv1) :: "memory");
  {
    const __amdgpu_buffer_rsrc_t yrsrc = __builtin_amdgcn_make_buffer_rsrc((void*)yaT, 0, D_ * S_ * 2, 0x00020000);
    const unsigned yo = (unsigned)(d0 * (S_ * 2) + (t0 + 16 * w) * 2) + vyo;
    unsigned spins = 0;
    while (true) {
      const bool ok = yv0.x != ~0u && yv0.y != ~0u && yv0.z != ~0u && yv0.w != ~0u &&
                      yv1.x != ~0u && yv1.y != ~0u && yv1.z != ~0u && yv1.w != ~0u;
      if (__all(ok) || ++spins > (1u << 16)) break;
      __builtin_amdgcn_s_sleep(2);
      yv0 = __builtin_amdgcn_raw_buffer_load_b128(yrsrc, yo, 0, 16);
      yv1 = __builtin_amdgcn_raw_buffer_load_b128(yrsrc, yo, 32 * S_ * 2, 16);
    }
  }
  f32x2 av[4][2];
  {
    char* yw = sm + 49152 + w * 3072 + (lane >> 1) * 48 + (lane & 1) * 16;
    *(v4u*)(yw) = yv0;
    *(v4u*)(yw + 32 * 48) = yv1;
    const char* yp = sm + 49152 + w * 3072 + (4 * q + (r16 >> 2)) * 48 + (lane & 3) * 8;
    asm volatile("s_waitcnt lgkmcnt(0)" ::: "memory");
    __builtin_amdgcn_s_barrier();
#pragma unroll
    for (int m = 0; m < 4; ++m) {
      const v4s yv = __builtin_amdgcn_ds_read_tr16_b64_v4i16((LAS v4s*)(yp + m * (16 * 48)));
      const v2u yu = __builtin_bit_cast(v2u, yv);
      av[m][0] = rb2(up2(xvv[m].x) + rb2(up2(xnn[m].x) * up2(yu.x)));
      av[m][1] = rb2(up2(xvv[m].y) + rb2(up2(xnn[m].y) * up2(yu.y)));
    }
  }
  {
    const float rs = __uint_as_float(svu.y), rs2 = rs * rs;
    const f32x2 r2 = {rs2, rs2};
    const char* rp = sm + (w * 3) * 4096 + lane * 16;
    char* op = (char*)out + (size_t)(t0 + 16 * w + r16) * 1024 + (d0 + 4 * q) * 2;
#pragma unroll
    for (int m = 0; m < 4; ++m) {
      f32x4 o = acc[m][0];
      o += *(const f32x4*)(rp + m * 1024);
      o += *(const f32x4*)(rp + 4096 + m * 1024);
      o += *(const f32x4*)(rp + 8192 + m * 1024);
      *(v2u*)(op + m * 32) = (v2u){pk2v(av[m][0] + rb2((f32x2){o[0], o[1]} * r2)), pk2v(av[m][1] + rb2((f32x2){o[2], o[3]} * r2))};
    }
  }
}

extern "C" void kernel_launch(void* const* d_in, const int* in_sizes, int n_in, void* d_out, int out_size,
                              void* d_ws, size_t ws_size, hipStream_t stream) {
  const bf16* q = (const bf16*)d_in[0];
  const bf16* k = (const bf16*)d_in[1];
  const bf16* v = (const bf16*)d_in[2];
  const bf16* sk = (const bf16*)d_in[3];
  const bf16* ssk = (const bf16*)d_in[4];
  const bf16* sD = (const bf16*)d_in[5];
  const bf16* wg = (const bf16*)d_in[7];
  const bf16* wu = (const bf16*)d_in[8];
  const bf16* wd = (const bf16*)d_in[9];
  char* ws = (char*)d_ws;
  float* stats = (float*)(ws + 4096);
  bf16* kvT = (bf16*)(ws + 65536);
  bf16* yaT = (bf16*)(ws + 65536 + (size_t)2 * 1024 * 1024);
  bf16* h = (bf16*)(ws + 65536 + (size_t)4 * 1024 * 1024);
  bf16* wdf = (bf16*)(ws + 65536 + (size_t)10 * 1024 * 1024);
  (void)hipFuncSetAttribute((const void*)k1_gemm1, hipFuncAttributeMaxDynamicSharedMemorySize, K1_LDS);
  (void)hipFuncSetAttribute((const void*)k2_conv_gemm2, hipFuncAttributeMaxDynamicSharedMemorySize, K2_LDS);
  k1_gemm1<<<256, 512, K1_LDS, stream>>>(q, k, v, sk, wg, wu, h, kvT, stats, wd, yaT, wdf, ssk);
  k2_conv_gemm2<<<512, 256, K2_LDS, stream>>>(q, ssk, sD, wdf, h, kvT, yaT, stats, (bf16*)d_out);
}
```
